```python
import torch

def _softmax(x, axis=-1):
    return torch.softmax(x, dim=axis)
def _gelu(x):
    return torch.nn.functional.gelu(x, approximate="tanh")
def _silu(x):
    return torch.nn.functional.silu(x)
def _sigmoid(x):
    return torch.sigmoid(x)
def _layernorm(x, axis=-1, eps=1e-5):
    m = x.mean(dim=axis, keepdim=True)
    v = x.var(dim=axis, keepdim=True, unbiased=False)
    return (x - m) / torch.sqrt(v + eps)
def _scatter_add(tgt, idx, src, axis=0):
    return tgt.clone().index_add_(axis, idx, src)
def _segment_sum(src, seg, n):
    out = torch.zeros((n,) + src.shape[1:], dtype=src.dtype)
    return out.index_add_(0, seg, src)
def _pad(x, pw):
    flat = tuple(v for pair in reversed(pw) for v in pair)
    return torch.nn.functional.pad(x, flat)

kernel_name = 'gated_mix__softmax_full_mha__dense_activation_silu__prompt'

def setup_inputs():

    _w = (lambda a: (lambda t: t.to(torch.bfloat16) if t.is_floating_point() else t)(torch.as_tensor(a)))
    def _A_setup():
        B, S, D = 1, 2048, 640
        return {
            "x": _w((lambda *s: torch.randn(*s, dtype=torch.bfloat16) * 0.1)(B, S, D)),
            "wqkv": _w((lambda *s: torch.randn(*s, dtype=torch.bfloat16) * 0.4)(D, 3 * D)),
            "wo": _w((lambda *s: torch.randn(*s, dtype=torch.bfloat16) * 0.2)(D, D)),
        }
    _A_in = _A_setup()

    def _B_setup():
        B, S, D, F = 1, 2048, 640, 1536
        return {
            "x": _w((lambda *s: torch.randn(*s, dtype=torch.bfloat16) * 0.1)(B, S, D)),
            "w1": _w((lambda *s: torch.randn(*s, dtype=torch.bfloat16) * 3.2)(D, F) * (D ** -0.5)),
            "w2": _w((lambda *s: torch.randn(*s, dtype=torch.bfloat16) * 3.2)(F, D) * (F ** -0.5)),
        }
    _B_in = _B_setup()

    return {
        "A_x": _A_in["x"],
        "A_wqkv": _A_in["wqkv"],
        "A_wo": _A_in["wo"],
        "B_x": _B_in["x"],
        "B_w1": _B_in["w1"],
        "B_w2": _B_in["w2"],
        "gate_w": _w((lambda *s: torch.randn(*s, dtype=torch.bfloat16) * 0.1)(640) * 16.0),
    }

def reference(A_x, A_wqkv, A_wo, B_x, B_w1, B_w2, gate_w):
    def _A_ref(x, wqkv, wo):
        B, S, D = x.shape
        H = 32; Dh = D // H
        qkv = (lambda x, *d: x.permute(*d))((x @ wqkv).reshape(B, S, 3, H, Dh), 2, 0, 3, 1, 4)
        q, k, v = qkv[0], qkv[1], qkv[2]
        scale = Dh ** -0.5
        scores = torch.einsum("bhqd,bhkd->bhqk", q, k) * scale
        p = _softmax(scores, axis=-1)
        o = torch.einsum("bhqk,bhkd->bhqd", p, v)
        return (lambda x, *d: x.permute(*d))(o, 0, 2, 1, 3).reshape(B, S, D) @ wo

    def _B_ref(x, w1, w2):
        h = torch.einsum("bsd,df->bsf", x, w1)
        h = _silu(h)
        out = torch.einsum("bsf,fd->bsd", h, w2)
        return out

    x = A_x
    g = _sigmoid((x * gate_w).sum(-1, keepdims=True))
    a = _A_ref(x, A_wqkv, A_wo)
    b = _B_ref(x, B_w1, B_w2)
    return g * a + (1 - g) * b
```

```cpp
#include <hip/hip_runtime.h>
#include <hip/hip_bf16.h>
#include <stdint.h>

typedef __bf16 bf16x8 __attribute__((ext_vector_type(8)));
typedef __bf16 bf16x2 __attribute__((ext_vector_type(2)));
typedef short s16x4 __attribute__((ext_vector_type(4)));
typedef float f32x16 __attribute__((ext_vector_type(16)));
typedef float f32x2 __attribute__((ext_vector_type(2)));
typedef unsigned u32x4 __attribute__((ext_vector_type(4)));
typedef unsigned u32x2 __attribute__((ext_vector_type(2)));
#define DI __device__ __forceinline__
#define LAS __attribute__((address_space(3)))
typedef LAS const char* lds_cptr;

constexpr int S_ = 2048, D_ = 640, F_ = 1536, H_ = 32;
constexpr float QSCALE = 0.22360679774997896f * 1.4426950408889634f;

constexpr size_t KCH = 5120, VCH = 6144;
constexpr size_t WS_Q = 0;
constexpr size_t WS_K = WS_Q + 32 * 16 * KCH;
constexpr size_t WS_V = WS_K + 32 * 16 * KCH;
constexpr size_t WS_H = WS_V + 32 * 16 * VCH;
constexpr size_t WS_O = WS_H + (size_t)S_ * F_ * 2;
constexpr size_t WS_END = WS_O + (size_t)S_ * D_ * 2;

DI unsigned cvtpk(float lo, float hi) { f32x2 v = {lo, hi}; bf16x2 b = __builtin_convertvector(v, bf16x2); return __builtin_bit_cast(unsigned, b); }
DI f32x16 mfma32(bf16x8 a, bf16x8 b, f32x16 c) { return __builtin_amdgcn_mfma_f32_32x32x16_bf16(a, b, c, 0, 0, 0); }
DI f32x16 mfma32k8(s16x4 a, s16x4 b, f32x16 c) { return __builtin_amdgcn_mfma_f32_32x32x8bf16_1k(a, b, c, 0, 0, 0); }
DI void glds16(unsigned voff, const void* sbase, unsigned ldsdst) {
  asm volatile("s_mov_b32 m0, %0\n\ts_nop 0\n\tglobal_load_lds_dwordx4 %1, %2" :: "s"(ldsdst), "v"(voff), "s"(sbase) : "memory", "m0");
}
typedef short v4i16_t __attribute__((ext_vector_type(4)));
DI s16x4 trread(lds_cptr p) { return __builtin_bit_cast(s16x4, __builtin_amdgcn_ds_read_tr16_b64_v4i16((LAS v4i16_t*)p)); }
template<int N> DI void wait_vm() { asm volatile("s_waitcnt vmcnt(%0)" :: "i"(N) : "memory"); }
DI void bar() { asm volatile("s_waitcnt lgkmcnt(0)\n\ts_barrier" ::: "memory"); }
DI unsigned rfl(unsigned x) { return __builtin_amdgcn_readfirstlane(x); }

template<int BM, int BN, bool AT, bool SWAP, int NST, int NK>
struct Gemm {
  static constexpr int TM = BM / 64, TN = BN / 64;
  static constexpr int ABYTES = BM * 128, BBYTES = BN * 128, STAGE = ABYTES + BBYTES;
  static constexpr int AI = BM / 32, BI = BN / 32;
  static constexpr int LPS = AI + BI;
  static constexpr int BROW = BN * 2;
  static constexpr int BCH = BN / 8;
  static DI int bswz(int k) { return BN == 128 ? ((k & 3) << 2) : (BN == 64 ? (((k >> 1) & 1) << 2) : 0); }

  static DI void run(const char* Abase, int lda_bytes  , const char* Bbase, int ldb_bytes, char* lds, f32x16 (&acc)[TM][TN]) {
    const int tid = threadIdx.x, lane = tid & 63, w = rfl(tid >> 6), wm = w >> 1, wn = w & 1;
    const int r32 = lane & 31, h = lane >> 5;
    const unsigned lds0 = (unsigned)(uintptr_t)lds;
    unsigned avoff[AI], bvoff[BI];
#pragma unroll
    for (int i = 0; i < AI; ++i) {
      const int j = w + 4 * i;
      if (AT) {
        avoff[i] = (j >> 2) * lda_bytes + (j & 3) * 1024 + lane * 16;
      } else {
        const int row = 8 * j + (lane >> 3), p = lane & 7, c = p ^ ((row >> 1) & 7);
        avoff[i] = row * lda_bytes + c * 16;
      }
    }
#pragma unroll
    for (int i = 0; i < BI; ++i) {
      const int j = w + 4 * i, idx = j * 64 + lane, k = idx / BCH, p = idx % BCH, c = p ^ bswz(k);
      bvoff[i] = k * ldb_bytes + c * 16;
    }
    auto issue = [&](int st, int slot) {
      const char* a = Abase + (AT ? st * 4096 : st * 128);
      const char* b = Bbase + (size_t)st * 64 * ldb_bytes;
#pragma unroll
      for (int i = 0; i < AI; ++i) glds16(avoff[i], a, lds0 + slot * STAGE + (w + 4 * i) * 1024);
#pragma unroll
      for (int i = 0; i < BI; ++i) glds16(bvoff[i], b, lds0 + slot * STAGE + ABYTES + (w + 4 * i) * 1024);
    };
    int aoff[4];
#pragma unroll
    for (int ks = 0; ks < 4; ++ks) {
      if (AT) aoff[ks] = (wm * TM) * 4096 + (2 * ks + h) * 512 + r32 * 16;
      else aoff[ks] = (wm * TM * 32 + r32) * 128 + (((2 * ks + h) ^ ((r32 >> 1) & 7)) << 4);
    }
    int boff[TN];
    { const int blk = (lane >> 4) & 1, q = (lane & 15) >> 2, p = lane & 3;
#pragma unroll
      for (int tn = 0; tn < TN; ++tn) {
        const int c0 = (wn * TN + tn) * 4 + 2 * blk + (p >> 1);
        boff[tn] = ABYTES + (8 * h + q) * BROW + ((c0 ^ bswz(q)) << 4) + 8 * (p & 1);
      } }
#pragma unroll
    for (int tm = 0; tm < TM; ++tm)
#pragma unroll
      for (int tn = 0; tn < TN; ++tn) acc[tm][tn] = f32x16{};
#pragma unroll
    for (int s = 0; s < NST - 1; ++s) if (s < NK) issue(s, s);
#pragma unroll
    for (int s = 0; s < NK; ++s) {
      constexpr int dummy = 0; (void)dummy;
      const int fl = (NK - 1 - s) < (NST - 2) ? (NK - 1 - s) : (NST - 2);
      if (fl >= 3) wait_vm<3 * LPS>(); else if (fl == 2) wait_vm<2 * LPS>(); else if (fl == 1) wait_vm<LPS>(); else wait_vm<0>();
      bar();
      if (s + NST - 1 < NK) issue(s + NST - 1, (s + NST - 1) % NST);
      const lds_cptr base = (lds_cptr)lds + (s % NST) * STAGE;
#pragma unroll
      for (int ks = 0; ks < 4; ++ks) {
        bf16x8 af[TM], bf[TN];
#pragma unroll
        for (int tm = 0; tm < TM; ++tm) af[tm] = *(const LAS bf16x8*)(base + aoff[ks] + tm * 4096);
#pragma unroll
        for (int tn = 0; tn < TN; ++tn) {
          s16x4 lo = trread(base + boff[tn] + (16 * ks) * BROW), hi = trread(base + boff[tn] + (16 * ks + 4) * BROW);
          bf[tn] = __builtin_bit_cast(bf16x8, __builtin_shufflevector(lo, hi, 0, 1, 2, 3, 4, 5, 6, 7));
        }
#pragma unroll
        for (int tm = 0; tm < TM; ++tm)
#pragma unroll
          for (int tn = 0; tn < TN; ++tn) acc[tm][tn] = SWAP ? mfma32(bf[tn], af[tm], acc[tm][tn]) : mfma32(af[tm], bf[tn], acc[tm][tn]);
      }
    }
  }
};

DI float silu_f(float v) { return v * __builtin_amdgcn_rcpf(1.f + __builtin_amdgcn_exp2f(-1.4426950408889634f * v)); }

__global__ __launch_bounds__(256, 2) void k_qkv_ffn1(const __hip_bfloat16* x, const __hip_bfloat16* wqkv, const __hip_bfloat16* w1, char* ws) {
  extern __shared__ __attribute__((aligned(16))) char lds[];
  const int b = blockIdx.x, tid = threadIdx.x, lane = tid & 63, w = rfl(tid >> 6), wm = w >> 1, wn = w & 1, r32 = lane & 31, h = lane >> 5;
  f32x16 acc[2][2];
  if (b < 240) {
    const int rb = b & 15, cb = b >> 4, part = cb / 5, n0 = (cb % 5) * 128;
    const char* A = (const char*)x + (size_t)rb * 128 * D_ * 2;
    const char* B = (const char*)wqkv + (size_t)(cb * 128) * 2;
    if (part < 2) {
      Gemm<128, 128, false, true, 2, 10>::run(A, D_ * 2, B, 1920 * 2, lds, acc);
      char* img = ws + (part == 0 ? WS_Q : WS_K) + (size_t)rb * KCH;
      const float sc = part == 0 ? QSCALE : 1.f;
#pragma unroll
      for (int tm = 0; tm < 2; ++tm)
#pragma unroll
        for (int tn = 0; tn < 2; ++tn)
#pragma unroll
          for (int j = 0; j < 4; ++j) {
            const int G = (n0 + wn * 64 + tn * 32) / 4 + 2 * j + h, head = G / 5, g = G - head * 5, tile = wm * 2 + tm;
            const unsigned lo = cvtpk(acc[tm][tn][4 * j] * sc, acc[tm][tn][4 * j + 1] * sc), hi = cvtpk(acc[tm][tn][4 * j + 2] * sc, acc[tm][tn][4 * j + 3] * sc);
            char* hb = img + (size_t)head * 16 * KCH;
            if (g < 4) *(u32x2*)(hb + tile * 1024 + (g >> 1) * 512 + r32 * 16 + (g & 1) * 8) = u32x2{lo, hi};
            else { *(unsigned*)(hb + 4096 + tile * 256 + r32 * 4) = lo; *(unsigned*)(hb + 4096 + tile * 256 + 128 + r32 * 4) = hi; }
          }
    } else {
      Gemm<128, 128, false, false, 2, 10>::run(A, D_ * 2, B, 1920 * 2, lds, acc);
      char* img = ws + WS_V + (size_t)rb * VCH;
#pragma unroll
      for (int tn = 0; tn < 2; ++tn) {
        const int n = n0 + wn * 64 + tn * 32 + r32, head = n / 20, d = n - head * 20;
        char* hb = img + (size_t)head * 16 * VCH + h * 384 + d * 16;
#pragma unroll
        for (int tm = 0; tm < 2; ++tm)
#pragma unroll
          for (int s = 0; s < 2; ++s) {
            const f32x16& a = acc[tm][tn];
            u32x4 v = {cvtpk(a[8 * s], a[8 * s + 1]), cvtpk(a[8 * s + 2], a[8 * s + 3]), cvtpk(a[8 * s + 4], a[8 * s + 5]), cvtpk(a[8 * s + 6], a[8 * s + 7])};
            char* p = hb + (wm * 2 + tm) * 1536 + s * 768;
            *(u32x4*)p = v;
            if (d == 19) *(u32x4*)(p + 16) = u32x4{0x3F803F80u, 0x3F803F80u, 0x3F803F80u, 0x3F803F80u};
          }
      }
    }
  } else {
    const int bb = b - 240, rb = bb & 15, cb = bb >> 4;
    const char* A = (const char*)x + (size_t)rb * 128 * D_ * 2;
    const char* B = (const char*)w1 + (size_t)(cb * 128) * 2;
    Gemm<128, 128, false, true, 2, 10>::run(A, D_ * 2, B, F_ * 2, lds, acc);
#pragma unroll
    for (int tm = 0; tm < 2; ++tm)
#pragma unroll
      for (int tn = 0; tn < 2; ++tn) {
        char* base = ws + WS_H + ((size_t)(rb * 4 + wm * 2 + tm) * (F_ / 8) + (cb * 128 + wn * 64 + tn * 32) / 8) * 512 + r32 * 16 + h * 8;
#pragma unroll
        for (int j = 0; j < 4; ++j) {
          const f32x16& a = acc[tm][tn];
          *(u32x2*)(base + j * 512) = u32x2{cvtpk(silu_f(a[4 * j]), silu_f(a[4 * j + 1])), cvtpk(silu_f(a[4 * j + 2]), silu_f(a[4 * j + 3]))};
        }
      }
  }
}

constexpr int ACH = 11264;
__global__ __launch_bounds__(256, 2) void k_attn(char* ws) {
  extern __shared__ __attribute__((aligned(16))) char lds[];
  const int b = blockIdx.x, head = b >> 3, qb = b & 7;
  const int tid = threadIdx.x, lane = tid & 63, w = rfl(tid >> 6), r32 = lane & 31, h = lane >> 5;
  const unsigned lds0 = (unsigned)(uintptr_t)lds;
  const char* Kg = ws + WS_K + (size_t)head * 16 * KCH;
  const char* Vg = ws + WS_V + (size_t)head * 16 * VCH;
  constexpr int NS = 3;
  auto issue = [&](int c, int slot) {
    const unsigned vo = lane * 16;
#pragma unroll
    for (int i = 0; i < 3; ++i) {
      const int p = w + 4 * i;
      if (p < 5) glds16(vo, Kg + (size_t)c * KCH + p * 1024, lds0 + slot * ACH + p * 1024);
      else if (p < 11) glds16(vo, Vg + (size_t)c * VCH + (p - 5) * 1024, lds0 + slot * ACH + p * 1024);
      else glds16(vo, Kg + (size_t)c * KCH, lds0 + NS * ACH);
    }
  };
  issue(0, 0); issue(1, 1);
  bf16x8 qm[2]; unsigned qt_[2];
#pragma unroll
  for (int qt = 0; qt < 2; ++qt) {
    const int row = qb * 256 + w * 64 + qt * 32, ch = row >> 7, tile = (row >> 5) & 3;
    const char* qp = ws + WS_Q + ((size_t)head * 16 + ch) * KCH;
    qm[qt] = *(const bf16x8*)(qp + tile * 1024 + lane * 16);
    qt_[qt] = *(const unsigned*)(qp + 4096 + tile * 256 + lane * 4);
  }
  const int voff = h * 384 + (r32 < 24 ? r32 : (r32 < 28 ? r32 - 4 : r32 - 12)) * 16;
  f32x16 o[2] = {f32x16{}, f32x16{}};
  unsigned bias[2] = {0u, 0u};
  const unsigned ONE2 = 0x3F803F80u;
  wait_vm<3>(); bar();
  {
    const lds_cptr kb = (lds_cptr)lds;
    bf16x8 km = *(const LAS bf16x8*)(kb + lane * 16);
    unsigned kt = *(const LAS unsigned*)(kb + 4096 + lane * 4);
    s16x4 ka = __builtin_bit_cast(s16x4, u32x2{kt, ONE2});
#pragma unroll
    for (int qt = 0; qt < 2; ++qt) {
      f32x16 s = mfma32(km, qm[qt], f32x16{});
      s = mfma32k8(ka, __builtin_bit_cast(s16x4, u32x2{qt_[qt], 0u}), s);
      float m = s[0];
#pragma unroll
      for (int i = 1; i < 16; ++i) m = fmaxf(m, s[i]);
      m = fmaxf(m, __shfl_xor(m, 32));
      const float nm = -m;
      const unsigned hi = cvtpk(nm, 0.f) & 0xffffu; const float r1 = nm - __uint_as_float(hi << 16);
      const unsigned mid = cvtpk(r1, 0.f) & 0xffffu; const float r2 = r1 - __uint_as_float(mid << 16);
      const unsigned lo = cvtpk(r2, 0.f) & 0xffffu;
      bias[qt] = h == 0 ? (hi | (mid << 16)) : lo;
    }
  }
  for (int c = 0; c < 16; ++c) {
    if (c > 0) { if (c < 15) wait_vm<3>(); else wait_vm<0>(); bar(); }
    if (c + 2 < 16) issue(c + 2, (c + 2) % NS);
    const lds_cptr kb = (lds_cptr)lds + (c % NS) * ACH;
#pragma unroll
    for (int t = 0; t < 4; ++t) {
      bf16x8 km = *(const LAS bf16x8*)(kb + t * 1024 + lane * 16);
      unsigned kt = *(const LAS unsigned*)(kb + 4096 + t * 256 + lane * 4);
      bf16x8 v0 = *(const LAS bf16x8*)(kb + 5120 + t * 1536 + voff);
      bf16x8 v1 = *(const LAS bf16x8*)(kb + 5120 + t * 1536 + 768 + voff);
      s16x4 ka = __builtin_bit_cast(s16x4, u32x2{kt, ONE2});
#pragma unroll
      for (int qt = 0; qt < 2; ++qt) {
        f32x16 s = mfma32(km, qm[qt], f32x16{});
        s = mfma32k8(ka, __builtin_bit_cast(s16x4, u32x2{qt_[qt], bias[qt]}), s);
        u32x4 p0, p1;
#pragma unroll
        for (int i = 0; i < 4; ++i) {
          p0[i] = cvtpk(__builtin_amdgcn_exp2f(s[2 * i]), __builtin_amdgcn_exp2f(s[2 * i + 1]));
          p1[i] = cvtpk(__builtin_amdgcn_exp2f(s[8 + 2 * i]), __builtin_amdgcn_exp2f(s[8 + 2 * i + 1]));
        }
        o[qt] = mfma32(v0, __builtin_bit_cast(bf16x8, p0), o[qt]);
        o[qt] = mfma32(v1, __builtin_bit_cast(bf16x8, p1), o[qt]);
      }
    }
  }
#pragma unroll
  for (int qt = 0; qt < 2; ++qt) {
    const float lsum = __shfl(o[qt][8], 32 + r32);
    const float inv = 1.f / lsum;
    const int rowtile = qb * 8 + w * 2 + qt;
    char* ob = ws + WS_O + (size_t)rowtile * 80 * 512 + r32 * 16;
#pragma unroll
    for (int j = 0; j < 3; ++j) {
      const int g = 2 * j + h;
      if (g < 5) {
        const int G = head * 5 + g;
        *(u32x2*)(ob + (G >> 1) * 512 + (G & 1) * 8) = u32x2{cvtpk(o[qt][4 * j] * inv, o[qt][4 * j + 1] * inv), cvtpk(o[qt][4 * j + 2] * inv, o[qt][4 * j + 3] * inv)};
      }
    }
  }
}

__global__ __launch_bounds__(256, 2) void k_final(const __hip_bfloat16* x, const __hip_bfloat16* gw, const __hip_bfloat16* wo, const __hip_bfloat16* w2, char* ws, __hip_bfloat16* out) {
  extern __shared__ __attribute__((aligned(16))) char lds[];
  __shared__ float gs[128];
  const int b = blockIdx.x, rb = b & 15, cb = b >> 4;
  const int tid = threadIdx.x, lane = tid & 63, w = rfl(tid >> 6), wm = w >> 1, wn = w & 1, r32 = lane & 31, h = lane >> 5;
  {
    const int row = tid >> 1, half = tid & 1;
    const bf16x8* xp = (const bf16x8*)(x + (size_t)(rb * 128 + row) * D_ + half * 320);
    const bf16x8* gp = (const bf16x8*)(gw + half * 320);
    float s = 0.f;
    for (int i = 0; i < 40; ++i) { bf16x8 a = xp[i], g = gp[i];
#pragma unroll
      for (int j = 0; j < 8; ++j) s += (float)a[j] * (float)g[j]; }
    s += __shfl_xor(s, 1);
    if (half == 0) gs[row] = 1.f / (1.f + __expf(-s));
  }
  f32x16 accb[2][1], acca[2][1];
  Gemm<128, 64, true, false, 3, 24>::run(ws + WS_H + (size_t)rb * 4 * (F_ / 8) * 512, (F_ / 8) * 512, (const char*)w2 + cb * 128, D_ * 2, lds, accb);
  bar();
  Gemm<128, 64, true, false, 3, 10>::run(ws + WS_O + (size_t)rb * 4 * 80 * 512, 80 * 512, (const char*)wo + cb * 128, D_ * 2, lds, acca);
#pragma unroll
  for (int tm = 0; tm < 2; ++tm)
#pragma unroll
    for (int r = 0; r < 16; ++r) {
      const int row = wm * 64 + tm * 32 + (r & 3) + 8 * (r >> 2) + 4 * h;
      const float g = gs[row];
      const float v = g * acca[tm][0][r] + (1.f - g) * accb[tm][0][r];
      out[(size_t)(rb * 128 + row) * D_ + cb * 64 + wn * 32 + r32] = __float2bfloat16(v);
    }
}

extern "C" void kernel_launch(void* const* d_in, const int* in_sizes, int n_in, void* d_out, int out_size, void* d_ws, size_t ws_size, hipStream_t stream) {
  const __hip_bfloat16* x = (const __hip_bfloat16*)d_in[0];
  const __hip_bfloat16* wqkv = (const __hip_bfloat16*)d_in[1];
  const __hip_bfloat16* wo = (const __hip_bfloat16*)d_in[2];
  const __hip_bfloat16* w1 = (const __hip_bfloat16*)d_in[4];
  const __hip_bfloat16* w2 = (const __hip_bfloat16*)d_in[5];
  const __hip_bfloat16* gw = (const __hip_bfloat16*)d_in[6];
  char* ws = (char*)d_ws;
  k_qkv_ffn1<<<432, 256, 65536, stream>>>(x, wqkv, w1, ws);
  k_attn<<<256, 256, 3 * ACH + 1024, stream>>>(ws);
  hipFuncSetAttribute((const void*)k_final, hipFuncAttributeMaxDynamicSharedMemorySize, 3 * (128 * 128 + 64 * 128));
  k_final<<<160, 256, 3 * (128 * 128 + 64 * 128), stream>>>(x, gw, wo, w2, ws, (__hip_bfloat16*)d_out);
}
```

```cpp
#define G_NODELAY 1
#include <hip/hip_runtime.h>
#include <hip/hip_bf16.h>
#include <stdint.h>

typedef __bf16 bf16x8 __attribute__((ext_vector_type(8)));
typedef __bf16 bf16x2 __attribute__((ext_vector_type(2)));
typedef short s16x4 __attribute__((ext_vector_type(4)));
typedef float f32x16 __attribute__((ext_vector_type(16)));
typedef float f32x4 __attribute__((ext_vector_type(4)));
typedef float f32x2 __attribute__((ext_vector_type(2)));
typedef unsigned u32x4 __attribute__((ext_vector_type(4)));
typedef unsigned u32x2 __attribute__((ext_vector_type(2)));
typedef unsigned long long u64;
#define DI __device__ __forceinline__
#define LAS __attribute__((address_space(3)))
#define GAS __attribute__((address_space(1)))
typedef LAS const char* lds_cptr;
typedef LAS char* lds_ptr;
typedef GAS u64 gu64;
typedef __amdgpu_buffer_rsrc_t rsrc_t;
#define RLX_AGENT __ATOMIC_RELAXED, __HIP_MEMORY_SCOPE_AGENT

constexpr int S_ = 2048, D_ = 640, F_ = 1536;
constexpr float QSCALE = 0.22360679774997896f * 1.4426950408889634f;
constexpr float LOG2E = 1.4426950408889634f;

constexpr unsigned WS_GEN = 0;
constexpr unsigned WS_ERR = 64;
constexpr unsigned WS_FKV = 4096;
constexpr unsigned WS_FH = 12288;
constexpr unsigned WS_FO = 14336;
constexpr unsigned WS_GATE = 16384;
constexpr unsigned KVCH = 6144;
constexpr unsigned KV_KT = 2048, KV_V = 2592, KV_USED = 5664;
constexpr unsigned WS_KV = 65536;
constexpr unsigned WS_H = WS_KV + 32 * 32 * KVCH;
constexpr unsigned WS_O = WS_H + S_ * F_ * 2;
constexpr unsigned WS_END = WS_O + S_ * D_ * 2;
constexpr int LDS_BYTES = 81920;

DI unsigned cvtpk(float lo, float hi) { f32x2 v = {lo, hi}; bf16x2 b = __builtin_convertvector(v, bf16x2); return __builtin_bit_cast(unsigned, b); }
DI f32x16 mfma32(bf16x8 a, bf16x8 b, f32x16 c) { return __builtin_amdgcn_mfma_f32_32x32x16_bf16(a, b, c, 0, 0, 0); }
DI f32x16 mfma32k8(s16x4 a, s16x4 b, f32x16 c) { return __builtin_amdgcn_mfma_f32_32x32x8bf16_1k(a, b, c, 0, 0, 0); }
DI f32x4 mfma16(bf16x8 a, bf16x8 b, f32x4 c) { return __builtin_amdgcn_mfma_f32_16x16x32_bf16(a, b, c, 0, 0, 0); }
DI rsrc_t mkrs(const void* p, unsigned bytes) { return __builtin_amdgcn_make_buffer_rsrc((void*)p, (short)0, (int)bytes, 0x00020000); }
DI void dma16(rsrc_t rs, unsigned voff, unsigned soff, unsigned ldsdst) {
  asm volatile("s_mov_b32 m0, %0\n\ts_nop 0\n\tbuffer_load_dwordx4 %1, %2, %3 offen lds" :: "s"(ldsdst), "v"(voff), "s"(rs), "s"(soff) : "memory", "m0");
}
DI void dma16_sc1(rsrc_t rs, unsigned voff, unsigned soff, unsigned ldsdst) {
  asm volatile("s_mov_b32 m0, %0\n\ts_nop 0\n\tbuffer_load_dwordx4 %1, %2, %3 offen sc1 lds" :: "s"(ldsdst), "v"(voff), "s"(rs), "s"(soff) : "memory", "m0");
}
DI void st16_sc1(rsrc_t rs, unsigned off, u32x4 v) { __builtin_amdgcn_raw_buffer_store_b128(v, rs, (int)off, 0, 16); }
typedef short v4i16_t __attribute__((ext_vector_type(4)));
DI s16x4 trread(lds_cptr p) { return __builtin_bit_cast(s16x4, __builtin_amdgcn_ds_read_tr16_b64_v4i16((LAS v4i16_t*)p)); }
DI bf16x8 cat8(s16x4 lo, s16x4 hi) { return __builtin_bit_cast(bf16x8, __builtin_shufflevector(lo, hi, 0, 1, 2, 3, 4, 5, 6, 7)); }
template <int N> DI void wait_vm() { asm volatile("s_waitcnt vmcnt(%0)" :: "i"(N) : "memory"); }
DI void bar() { asm volatile("s_waitcnt lgkmcnt(0)\n\ts_barrier" ::: "memory"); }
DI unsigned rfl(unsigned x) { return __builtin_amdgcn_readfirstlane(x); }
#define SBAR() __builtin_amdgcn_sched_barrier(0)
#define PIN(x) asm volatile("" : "+v"(x))
constexpr unsigned ONE2 = 0x3F803F80u;
constexpr unsigned SPIN_CAP = 40000;
#define PREFETCH_W2 1
#define PRIO_A 2
#define PRIO_G 0
#define ABL_NODMA 0
#define ABL_NOPIECE 0
#define ABL_ANY 0
#define ABL_NOBAR 0
#define ABL_NOLDS 0
#define TS(i) do {} while (0)
#define TC(i) do {} while (0)

DI unsigned poll_line(const char* ws, unsigned off, u64 gen, int lane) {
  const u64 v = __hip_atomic_load((gu64*)(ws + off + (lane & 15) * 8), RLX_AGENT);
  return (unsigned)__ballot(v == gen) & 0xffffu;
}
DI unsigned wait_line(const char* ws, unsigned off, u64 gen, int lane, unsigned needmask) {
  unsigned m = poll_line(ws, off, gen, lane);
  for (unsigned spins = 0; (m & needmask) != needmask; ++spins) {
    if (spins > SPIN_CAP) { if (lane == 0) __hip_atomic_store((gu64*)(ws + WS_ERR), gen, RLX_AGENT); return 0xffffu; }
    __builtin_amdgcn_s_sleep(2);
    m = poll_line(ws, off, gen, lane);
  }
  return m;
}
DI void publish(char* ws, unsigned off, u64 gen) {
  wait_vm<0>(); bar();
  if (threadIdx.x == 0) __hip_atomic_store((gu64*)(ws + off), gen, RLX_AGENT);
}

struct XLoad {
  rsrc_t rs; unsigned voff, sbase, ldst;
  DI void init(const void* x, int rb, int w, int lane, unsigned lds0) {
    rs = mkrs(x, S_ * D_ * 2);
    voff = (8 * w + (lane >> 3)) * 1280 + (((lane & 7) ^ ((4 * (w & 1) + (lane >> 4)) & 7)) << 4);
    sbase = rb * 128 * 1280; ldst = lds0 + w * 1024;
  }
  DI void issue1(int stage, int slot, int ii) const { dma16(rs, voff, sbase + ii * 32 * 1280 + stage * 128, ldst + slot * 16384 + ii * 4096); }
  DI void issue(int stage, int slot) const {
#pragma unroll
    for (int ii = 0; ii < 4; ++ii) dma16(rs, voff, sbase + ii * 32 * 1280 + stage * 128, ldst + slot * 16384 + ii * 4096);
  }
};

constexpr int qkv_wait(int tgt) {
  int pos = 0, posA[10] = {}, posB[20] = {};
  pos += 4; posA[0] = pos; pos += 2; posB[0] = pos; pos += 2; posB[1] = pos; pos += 4; posA[1] = pos; pos += 2; posB[2] = pos; pos += 2; posB[3] = pos; pos += 4; posA[2] = pos;
  for (int h = 0; h < tgt - 1; ++h) {
    if (h + 4 < 20) { pos += 2; posB[h + 4] = pos; }
    if ((h & 1) && (h + 5) / 2 < 10) pos += 2;
    if (!(h & 1) && h >= 2 && h / 2 + 2 < 10) { pos += 2; posA[h / 2 + 2] = pos; }
  }
  const int need = posB[tgt] > posA[tgt / 2] ? posB[tgt] : posA[tgt / 2];
  return pos - need;
}
struct KFrag { bf16x8 m; s16x4 t; };
struct VFrag { s16x4 a0, a1, b0, b1; };

struct GFrag { bf16x8 a[2][2], b[2][2]; };
template <int HS> DI void qkv_frags(GFrag& f, lds_cptr lds, const int (&aoff)[4], const int (&boff)[2]) {
  const lds_cptr ab = lds + ((HS / 2) % 3) * 16384, bb = lds + 49152 + (HS & 3) * 8192;
#pragma unroll
  for (int k2 = 0; k2 < 2; ++k2) {
#pragma unroll
    for (int tm = 0; tm < 2; ++tm) f.a[k2][tm] = *(const LAS bf16x8*)(ab + aoff[2 * (HS & 1) + k2] + tm * 4096);
#pragma unroll
    for (int tn = 0; tn < 2; ++tn) f.b[k2][tn] = cat8(trread(bb + boff[tn] + k2 * 16 * 256), trread(bb + boff[tn] + k2 * 16 * 256 + 4 * 256));
  }
}
template <int HS> DI void qkv_step(f32x16 (&acc)[2][2], GFrag (&fr)[2], const XLoad& xl, rsrc_t rw, unsigned bvoff, unsigned bldst, lds_cptr lds, const int (&aoff)[4], const int (&boff)[2]) {
  if constexpr (HS + 1 < 20) { wait_vm<qkv_wait(HS + 1)>(); bar(); }
  const GFrag& f = fr[HS & 1];
  SBAR();
  acc[0][0] = mfma32(f.b[0][0], f.a[0][0], acc[0][0]);
  SBAR(); if constexpr (HS + 1 < 20) qkv_frags<HS + 1>(fr[(HS + 1) & 1], lds, aoff, boff); SBAR();
  acc[0][1] = mfma32(f.b[0][1], f.a[0][0], acc[0][1]);
  SBAR(); if (HS + 4 < 20) dma16(rw, bvoff, (HS + 4) * 32 * 3840, bldst + (HS & 3) * 8192); SBAR();
  acc[1][0] = mfma32(f.b[0][0], f.a[0][1], acc[1][0]); acc[1][1] = mfma32(f.b[0][1], f.a[0][1], acc[1][1]);
  SBAR(); if (HS + 4 < 20) dma16(rw, bvoff, (HS + 4) * 32 * 3840 + 16 * 3840, bldst + (HS & 3) * 8192 + 4096); SBAR();
  acc[0][0] = mfma32(f.b[1][0], f.a[1][0], acc[0][0]); acc[0][1] = mfma32(f.b[1][1], f.a[1][0], acc[0][1]);
  constexpr int SA = (HS & 1) ? (HS + 5) / 2 : HS / 2 + 2; constexpr bool DOA = ((HS & 1) || HS >= 2) && SA < 10; constexpr int P0 = (HS & 1) ? 0 : 2;
  SBAR(); if (DOA) xl.issue1(SA, SA % 3, P0); SBAR();
  acc[1][0] = mfma32(f.b[1][0], f.a[1][1], acc[1][0]);
  SBAR(); if (DOA) xl.issue1(SA, SA % 3, P0 + 1); SBAR();
  acc[1][1] = mfma32(f.b[1][1], f.a[1][1], acc[1][1]);
  SBAR();
}
template <int... I> struct iseq {};
template <int N, int... I> struct mkseq : mkseq<N - 1, N - 1, I...> {};
template <int... I> struct mkseq<0, I...> { typedef iseq<I...> type; };
template <int... I> DI void qkv_all(iseq<I...>, f32x16 (&acc)[2][2], const XLoad& xl, rsrc_t rw, unsigned bvoff, unsigned bldst, lds_cptr lds, const int (&aoff)[4], const int (&boff)[2]) {
  GFrag fr[2];
  wait_vm<qkv_wait(0)>(); bar();
  qkv_frags<0>(fr[0], lds, aoff, boff);
  (qkv_step<I>(acc, fr, xl, rw, bvoff, bldst, lds, aoff, boff), ...);
}

constexpr int ARING = 3 * KVCH;
constexpr int QST = 2 * KVCH;
constexpr int XCH = 4 * ARING;
constexpr int DUMMY = XCH + 1024;

template <int IMM> DI void dma16_sc1_imm(rsrc_t rs, unsigned voff, unsigned soff, unsigned ldsdst) {
  asm volatile("s_mov_b32 m0, %0\n\ts_nop 0\n\tbuffer_load_dwordx4 %1, %2, %3 offen offset:%4 sc1 lds" :: "s"(ldsdst), "v"(voff), "s"(rs), "s"(soff), "i"(IMM) : "memory", "m0");
}

template <int WN, int TN, int J> DI void qkv_store(const f32x16 (&acc)[2][2], lds_ptr ldw, int wm, int r32, int h) {
  constexpr int F0 = WN * 16 + TN * 8 + 2 * J, F1 = F0 + 1;
  constexpr int P0 = F0 / 10, H0 = (F0 % 10) / 5, G0 = F0 % 5, P1 = F1 / 10, H1 = (F1 % 10) / 5, G1 = F1 % 5;
  auto off = [&](int P, int H, int G, int tm) -> int {
    if (P == 0) return 2 * H * ARING + QST + (G < 4 ? (wm * 2 + tm) * 1024 + (G >> 1) * 512 + (G & 1) * 8 + r32 * 16 : 4096 + (wm * 2 + tm) * 256 + r32 * 8);
    const int base = (2 * H + wm) * ARING;
    if (P == 1) return base + (G < 4 ? tm * 1024 + (G >> 1) * 512 + (G & 1) * 8 + r32 * 16 : (int)KV_KT + tm * 272 + r32 * 8);
    return base + (int)KV_V + (tm * 32 + r32) * 48 + G * 8;
  };
#pragma unroll
  for (int tm = 0; tm < 2; ++tm) {
    const f32x16& a = acc[tm][TN];
    const float sc = (h ? P1 : P0) == 0 ? QSCALE : 1.f;
    const u32x2 v = {cvtpk(a[4 * J] * sc, a[4 * J + 1] * sc), cvtpk(a[4 * J + 2] * sc, a[4 * J + 3] * sc)};
    if (P1 < 3) { const int o = h ? off(P1, H1, G1, tm) : off(P0, H0, G0, tm); *(LAS u32x2*)(ldw + o) = v; }
    else if (P0 < 3) { if (h == 0) *(LAS u32x2*)(ldw + off(P0, H0, G0, tm)) = v; }
    if (P0 == 2 && G0 == 4) { if (h == 0) *(LAS u32x2*)(ldw + off(P0, H0, G0, tm) + 8) = u32x2{0x00003F80u, 0u}; }
    if (P1 == 2 && G1 == 4) { if (h == 1) *(LAS u32x2*)(ldw + off(P1, H1, G1, tm) + 8) = u32x2{0x00003F80u, 0u}; }
  }
}
template <int WN> DI void qkv_store_all(const f32x16 (&acc)[2][2], lds_ptr ldw, int wm, int r32, int h) {
  qkv_store<WN, 0, 0>(acc, ldw, wm, r32, h); qkv_store<WN, 0, 1>(acc, ldw, wm, r32, h); qkv_store<WN, 0, 2>(acc, ldw, wm, r32, h); qkv_store<WN, 0, 3>(acc, ldw, wm, r32, h);
  qkv_store<WN, 1, 0>(acc, ldw, wm, r32, h); qkv_store<WN, 1, 1>(acc, ldw, wm, r32, h); qkv_store<WN, 1, 2>(acc, ldw, wm, r32, h); qkv_store<WN, 1, 3>(acc, ldw, wm, r32, h);
}

DI void role_a(const void* x, const void* wqkv, char* ws, char* ldsg, int b, u64 gen) {
  const int tid = threadIdx.x, lane = tid & 63, w = rfl(tid >> 6), r32 = lane & 31, h = lane >> 5;
  const int j8 = b & 7, bi = b >> 3, rg = j8 & 3, rb = rg * 4 + (bi & 3), pr = (j8 >> 2) * 8 + (bi >> 2);
  const unsigned lds0 = (unsigned)(uintptr_t)ldsg;
  const lds_cptr lds = (lds_cptr)ldsg; const lds_ptr ldw = (lds_ptr)ldsg;
  const rsrc_t rws = mkrs(ws, WS_END);
  const int hh = w >> 1, kh = w & 1, head = 2 * pr + hh;
  const int ring = w * ARING;
  {
    const int wm = w >> 1, wn = w & 1;
    XLoad xl; xl.init(x, rb, w, lane, lds0);
    const rsrc_t rw = mkrs(wqkv, D_ * 1920 * 2);
    int p = (lane & 15) ^ (((lane >> 4) & 3) << 2); if (p == 15) p = 14;
    const unsigned bvoff = (4 * w + (lane >> 4)) * 3840 + ((p / 5) * 640 + 40 * pr + (p % 5) * 8) * 2;
    const unsigned bldst = lds0 + 49152 + w * 1024;
    xl.issue(0, 0);
    dma16(rw, bvoff, 0, bldst); dma16(rw, bvoff, 16 * 3840, bldst + 4096);
    dma16(rw, bvoff, 32 * 3840, bldst + 8192); dma16(rw, bvoff, 48 * 3840, bldst + 8192 + 4096);
    xl.issue(1, 1);
    dma16(rw, bvoff, 64 * 3840, bldst + 16384); dma16(rw, bvoff, 80 * 3840, bldst + 16384 + 4096);
    dma16(rw, bvoff, 96 * 3840, bldst + 24576); dma16(rw, bvoff, 112 * 3840, bldst + 24576 + 4096);
    xl.issue(2, 2);
    int aoff[4], boff[2];
#pragma unroll
    for (int ks = 0; ks < 4; ++ks) aoff[ks] = (wm * 64 + r32) * 128 + (((2 * ks + h) ^ ((r32 >> 1) & 7)) << 4);
    { const int blk = (lane >> 4) & 1, q = (lane & 15) >> 2, pp = lane & 3;
#pragma unroll
      for (int tn = 0; tn < 2; ++tn) boff[tn] = (8 * h + q) * 256 + (((wn * 8 + tn * 4 + 2 * blk + (pp >> 1)) ^ (q << 2)) << 4) + 8 * (pp & 1); }
    f32x16 acc[2][2] = {{f32x16{}, f32x16{}}, {f32x16{}, f32x16{}}};
    qkv_all(mkseq<20>::type{}, acc, xl, rw, bvoff, bldst, lds, aoff, boff);
    bar();
    TS(1);
    if (wn == 0) qkv_store_all<0>(acc, ldw, wm, r32, h); else qkv_store_all<1>(acc, ldw, wm, r32, h);
    if (lane < 2) *(LAS u32x2*)(ldw + ring + KV_KT + lane * 272 + 256) = u32x2{ONE2, 0x00003F80u};
    bar();
    TS(6);
#pragma unroll
    for (int i = 0; i < 6; ++i) { const int o = i * 1024 + lane * 16;
      if (o < (int)KV_USED) st16_sc1(rws, WS_KV + (head * 32 + rb * 2 + kh) * KVCH + o, *(const LAS u32x4*)(lds + ring + o)); }
    TS(7);
  }
  __builtin_amdgcn_s_setprio(PRIO_A);
  const unsigned fline = WS_FKV + (pr * 4 + w) * 128;
  unsigned ready = 1u << rb;
  auto chunk_of = [&](int n) { return (((rg + (n >> 2)) & 3) << 2) | ((rb + n) & 3); };
  auto need = [&](int n) { const int ch = chunk_of(n); if (!((ready >> ch) & 1)) ready |= wait_line(ws, fline, gen, lane, 1u << ch); };
  const unsigned l16 = lane * 16;
  const char* fptr = ws + fline + (lane & 15) * 8;
#define DMA_PIECE(n, so, i) do { const unsigned g_ = WS_KV + (head * 32 + chunk_of(n) * 2 + kh) * KVCH + ((i) >> 2) * 4096; const unsigned d_ = lds0 + ring + (so) + ((i) >> 2) * 4096; \
    dma16_sc1_imm<((i) & 3) * 1024>(rws, l16, g_, d_); } while (0)
#define DMA_ALL(n, so) do { DMA_PIECE(n, so, 0); DMA_PIECE(n, so, 1); DMA_PIECE(n, so, 2); DMA_PIECE(n, so, 3); DMA_PIECE(n, so, 4); DMA_PIECE(n, so, 5); } while (0)
  bf16x8 qm[4]; s16x4 qa[4]; u32x2 qtl[4];
#pragma unroll
  for (int qt = 0; qt < 4; ++qt) {
    qm[qt] = *(const LAS bf16x8*)(lds + 2 * hh * ARING + QST + qt * 1024 + lane * 16);
    qtl[qt] = *(const LAS u32x2*)(lds + 2 * hh * ARING + QST + 4096 + qt * 256 + r32 * 8);
    if (h) qtl[qt] = u32x2{0u, 0u};
  }
  const lds_cptr lk = lds + ring + lane * 16, lt = lds + ring + KV_KT + (h ? 256 : r32 * 8);
  const lds_cptr lv = lds + ring + KV_V + (4 * h + ((lane & 15) >> 2)) * 48 + 32 * ((lane >> 4) & 1) + 8 * (lane & 3);
  auto lK = [&](int so, int t) { if (ABL_NOLDS) { KFrag k; k.m = qm[0]; k.t = __builtin_bit_cast(s16x4, qtl[0]); PIN(k.m); PIN(k.t); return k; } KFrag k; k.m = *(const LAS bf16x8*)(lk + so + t * 1024); k.t = *(const LAS s16x4*)(lt + so + t * 272); return k; };
  auto lV = [&](int so, int t) { if (ABL_NOLDS) { VFrag v; v.a0 = v.a1 = v.b0 = v.b1 = __builtin_bit_cast(s16x4, qtl[1]); PIN(v.a0); PIN(v.a1); PIN(v.b0); PIN(v.b1); return v; } VFrag v; const lds_cptr p = lv + so + t * 1536; v.a0 = trread(p); v.a1 = trread(p + 8 * 48); v.b0 = trread(p + 16 * 48); v.b1 = trread(p + 24 * 48); return v; };
  bar();
  f32x16 o[4]; float mref[4];
  bool exact = false;
  for (int attempt = 0; attempt < 2; ++attempt) {
    float mrow[4];
    if (!exact) {
      const KFrag k0 = lK(0, 0);
#pragma unroll
      for (int qt = 0; qt < 4; ++qt) {
        f32x16 s = mfma32k8(k0.t, __builtin_bit_cast(s16x4, qtl[qt]), mfma32(k0.m, qm[qt], f32x16{}));
        float m = s[0];
#pragma unroll
        for (int i = 1; i < 16; ++i) m = fmaxf(m, s[i]);
        mrow[qt] = fmaxf(m, __shfl_xor(m, 32));
      }
    } else {
#pragma unroll
      for (int qt = 0; qt < 4; ++qt) mrow[qt] = -3.0e38f;
      for (int n = 0; n < 16; ++n) {
        asm volatile("s_waitcnt lgkmcnt(0)" ::: "memory");
        need(n); DMA_ALL(n, 0); wait_vm<0>();
        for (int t = 0; t < 2; ++t) {
          const KFrag k0 = lK(0, t);
#pragma unroll
          for (int qt = 0; qt < 4; ++qt) {
            f32x16 s = mfma32k8(k0.t, __builtin_bit_cast(s16x4, qtl[qt]), mfma32(k0.m, qm[qt], f32x16{}));
#pragma unroll
            for (int i = 0; i < 16; ++i) mrow[qt] = fmaxf(mrow[qt], s[i]);
          }
        }
      }
#pragma unroll
      for (int qt = 0; qt < 4; ++qt) mrow[qt] = fmaxf(mrow[qt], __shfl_xor(mrow[qt], 32));
      asm volatile("s_waitcnt lgkmcnt(0)" ::: "memory");
      DMA_ALL(0, 0); wait_vm<0>();
    }
#pragma unroll
    for (int qt = 0; qt < 4; ++qt) {
      const float nm = -mrow[qt];
      const unsigned hi = cvtpk(nm, 0.f) & 0xffffu; const float r1 = nm - __uint_as_float(hi << 16);
      const unsigned mid = cvtpk(r1, 0.f) & 0xffffu; const float r2 = r1 - __uint_as_float(mid << 16);
      const unsigned lo = cvtpk(r2, 0.f) & 0xffffu;
      mref[qt] = -(__uint_as_float(hi << 16) + __uint_as_float(mid << 16) + __uint_as_float(lo << 16));
      qa[qt] = __builtin_bit_cast(s16x4, h ? u32x2{hi | (mid << 16), lo} : qtl[qt]);
      o[qt] = f32x16{};
    }
#define EXP2(x) __builtin_amdgcn_exp2f(x)
#define EXI(i) S[cur][i] = EXP2(S[cur][i])
#define CVI(j) P[cur][(j) >> 2][(j) & 3] = cvtpk(S[cur][2 * (j)], S[cur][2 * (j) + 1])
#define HALF(cur_, KN, qn, VP, qp, X0, X1, X2, X3) do { constexpr int cur = cur_; \
    SBAR(); \
    P[cur ^ 1][1][2] = cvtpk(S[cur ^ 1][12], S[cur ^ 1][13]); P[cur ^ 1][1][3] = cvtpk(S[cur ^ 1][14], S[cur ^ 1][15]); PIN(P[cur ^ 1][1]); \
    SBAR(); \
    S[cur ^ 1] = mfma32(KN.m, qm[qn], f32x16{}); SBAR(); X0; \
    EXI(0); EXI(1); EXI(2); EXI(3); EXI(4); EXI(5); PIN(S[cur]); \
    SBAR(); \
    S[cur ^ 1] = mfma32k8(KN.t, qa[qn], S[cur ^ 1]); SBAR(); X1; \
    CVI(0); EXI(6); CVI(1); EXI(7); EXI(8); EXI(9); PIN(S[cur]); PIN(P[cur][0]); \
    SBAR(); \
    o[qp] = mfma32(cat8(VP.a0, VP.a1), __builtin_bit_cast(bf16x8, P[cur ^ 1][0]), o[qp]); SBAR(); X2; \
    CVI(2); EXI(10); CVI(3); EXI(11); EXI(12); EXI(13); PIN(S[cur]); PIN(P[cur][0]); \
    SBAR(); \
    o[qp] = mfma32(cat8(VP.b0, VP.b1), __builtin_bit_cast(bf16x8, P[cur ^ 1][1]), o[qp]); SBAR(); X3; \
    CVI(4); EXI(14); CVI(5); EXI(15); PIN(S[cur]); PIN(P[cur][1]); \
    SBAR(); \
  } while (0)
    TS(3); TC(8);
    for (int phase = 0; phase < 2; ++phase) {
      const int n0 = phase, n1 = phase ? 16 : 1;
      int sc = phase * KVCH, sn = (phase + 1) * KVCH, sp = phase ? 0 : 2 * KVCH;
      if (phase) {
        TC(10);
        if (attempt == 0) { wait_vm<0>(); if (lane == 0) __hip_atomic_store((gu64*)(ws + fline + rb * 8), gen, RLX_AGENT); TS(2); }
        need(1); DMA_ALL(1, KVCH); need(2); DMA_ALL(2, 2 * KVCH); wait_vm<6>(); TC(11);
      }
      f32x16 S[2]; u32x4 P[2][2]; KFrag kf[2]; VFrag vf[2];
      kf[0] = lK(sc, 0);
      vf[1] = lV(sc, 0);
      S[0] = mfma32k8(kf[0].t, qa[0], mfma32(kf[0].m, qm[0], f32x16{}));
      S[1] = f32x16{};
      P[1][0] = u32x4{0, 0, 0, 0}; P[1][1] = u32x4{0, 0, 0, 0};
      for (int n = n0; n < n1; ++n) {
        const bool pf = !ABL_NODMA && n + 2 < n1;
        if (__builtin_expect(pf && !((ready >> chunk_of(n + 2)) & 1), 0)) need(n + 2);
        const unsigned g0 = pf ? WS_KV + (head * 32 + chunk_of(n + 2) * 2 + kh) * KVCH : 0x7f000000u, g1 = g0 + 4096;
        const unsigned d0 = pf ? lds0 + ring + sp : lds0 + DUMMY, d1 = d0 + 4096;
#define PIECE_M0(IMM, g, d) if (!ABL_NOPIECE) asm volatile("s_mov_b32 m0, %0\n\ts_nop 0\n\tbuffer_load_dwordx4 %1, %2, %3 offen offset:" #IMM " sc1 lds" :: "s"(d), "v"(l16), "s"(rws), "s"(g) : "memory", "m0")
#define PIECE(IMM, g) if (!ABL_NOPIECE) asm volatile("buffer_load_dwordx4 %0, %1, %2 offen offset:" #IMM " sc1 lds" :: "v"(l16), "s"(rws), "s"(g) : "memory")
        HALF(0, kf[0], 1, vf[1], 3, (vf[0] = lV(sc, 0)), (void)0, (void)0, PIECE_M0(0, g0, d0));
        HALF(1, kf[0], 2, vf[0], 0, (kf[1] = lK(sc, 1)), (void)0, (void)0, PIECE(1024, g0));
        HALF(0, kf[0], 3, vf[0], 1, (void)0, (void)0, (void)0, PIECE(2048, g0));
        HALF(1, kf[1], 0, vf[0], 2, (void)0, (void)0, (void)0, PIECE(3072, g0));
        HALF(0, kf[1], 1, vf[0], 3, (vf[1] = lV(sc, 1)), (void)0, (void)0, PIECE_M0(0, g1, d1));
        if (__builtin_expect(phase != 0, 1)) wait_vm<5>();
        HALF(1, kf[1], 2, vf[1], 0, (kf[0] = lK(sn, 0)), (void)0, (void)0, PIECE(1024, g1));
        HALF(0, kf[1], 3, vf[1], 1, (void)0, (void)0, (void)0, (void)0);
        HALF(1, kf[0], 0, vf[1], 2, (void)0, (void)0, (void)0, (void)0);
        const int tmp = sc; sc = sn; sn = sp; sp = tmp;
      }
      wait_vm<0>();
      P[1][1][2] = cvtpk(S[1][12], S[1][13]); P[1][1][3] = cvtpk(S[1][14], S[1][15]);
      o[3] = mfma32(cat8(vf[1].a0, vf[1].a1), __builtin_bit_cast(bf16x8, P[1][0]), o[3]);
      o[3] = mfma32(cat8(vf[1].b0, vf[1].b1), __builtin_bit_cast(bf16x8, P[1][1]), o[3]);
    }
    TS(4); TC(9);
    if (exact || ABL_ANY) break;
    const bool bad = h == 1 && !(o[0][8] <= 1.0e9f && o[1][8] <= 1.0e9f && o[2][8] <= 1.0e9f && o[3][8] <= 1.0e9f);
    if (tid == 0) *(LAS unsigned*)(ldw + XCH) = 0u;
    bar();
    if (__any(bad) && lane == 0) *(LAS unsigned*)(ldw + XCH) = 1u;
    bar();
    if (rfl(*(const LAS unsigned*)(lds + XCH)) == 0u) break;
    exact = true;
  }
  __builtin_amdgcn_s_setprio(0);
  bar();
  auto send = [&](const f32x16& a, float m, int i) {
    const lds_ptr p = ldw + 16384 + (w * 2 + i) * 4096 + lane * 16;
    *(LAS f32x4*)(p) = f32x4{a[0], a[1], a[2], a[3]}; *(LAS f32x4*)(p + 1024) = f32x4{a[4], a[5], a[6], a[7]};
    *(LAS f32x4*)(p + 2048) = f32x4{a[8], a[9], a[10], a[11]}; *(LAS float*)(p + 3072) = m;
  };
  auto fin = [&](const f32x16& a, float ma, int i, int qt) {
    const lds_cptr p = lds + 16384 + ((w ^ 1) * 2 + i) * 4096 + lane * 16;
    const f32x4 b0 = *(const LAS f32x4*)(p), b1 = *(const LAS f32x4*)(p + 1024), b2 = *(const LAS f32x4*)(p + 2048);
    const float mb = *(const LAS float*)(p + 3072);
    const float M = fmaxf(ma, mb), fa = EXP2(ma - M), fb = EXP2(mb - M);
    float v[12];
#pragma unroll
    for (int e = 0; e < 4; ++e) { v[e] = fa * a[e] + fb * b0[e]; v[4 + e] = fa * a[4 + e] + fb * b1[e]; v[8 + e] = fa * a[8 + e] + fb * b2[e]; }
    const float inv = 1.f / __shfl(v[8], 32 + r32);
#pragma unroll
    for (int j = 0; j < 3; ++j) {
      const int g = 2 * j + h, G = hh * 5 + g;
      if (g < 5) *(LAS u32x2*)(ldw + qt * 2560 + (G >> 1) * 512 + r32 * 16 + (G & 1) * 8) = u32x2{cvtpk(v[4 * j] * inv, v[4 * j + 1] * inv), cvtpk(v[4 * j + 2] * inv, v[4 * j + 3] * inv)};
    }
  };
  if (kh == 0) { send(o[2], mref[2], 0); send(o[3], mref[3], 1); } else { send(o[0], mref[0], 0); send(o[1], mref[1], 1); }
  bar();
  if (kh == 0) { fin(o[0], mref[0], 0, 0); fin(o[1], mref[1], 1, 1); } else { fin(o[2], mref[2], 0, 2); fin(o[3], mref[3], 1, 3); }
  bar();
#pragma unroll
  for (int it = 0; it < 3; ++it) {
    const int pc = it * 256 + tid;
    if (pc < 640) { const int rt = pc / 160, o2 = (pc - rt * 160) * 16;
      st16_sc1(rws, WS_O + ((rb * 4 + rt) * 80 + pr * 5) * 512 + o2, *(const LAS u32x4*)(lds + pc * 16)); }
  }
  publish(ws, WS_FO + rb * 128 + pr * 8, gen);
  TS(5);
}

DI float silu_f(float v) { return v * __builtin_amdgcn_rcpf(1.f + __builtin_amdgcn_exp2f(-LOG2E * v)); }
#define LD16(d, rs, voff, soff, IMM) asm volatile("buffer_load_dwordx4 %0, %1, %2, %3 offen offset:" #IMM " sc1" : "=v"(d) : "v"(voff), "s"(rs), "s"(soff) : "memory")

template <int ST> DI void ffn1_step(f32x16 (&acc)[2][3], const XLoad& xl, rsrc_t rw, const unsigned (&bvoff)[3], unsigned bldst, lds_cptr lds, const int (&aoff)[2], int boff) {
  if (ST + 1 < 10) wait_vm<4>(); else wait_vm<0>();
  bar();
  if (ST + 1 < 10) {
#pragma unroll
    for (int i = 0; i < 3; ++i) dma16(rw, bvoff[i], (ST + 1) * 64 * 3072, bldst + ((ST + 1) & 1) * 12288 + i * 4096);
  }
  if (ST + 2 < 10) xl.issue(ST + 2, (ST + 2) % 3);
  const lds_cptr ab = lds + (ST % 3) * 16384, bb = lds + 49152 + (ST & 1) * 12288;
#pragma unroll
  for (int k2 = 0; k2 < 2; ++k2) {
    bf16x8 af[2], bf[3];
#pragma unroll
    for (int tm = 0; tm < 2; ++tm) af[tm] = *(const LAS bf16x8*)(ab + aoff[k2] + tm * 4096);
#pragma unroll
    for (int tn = 0; tn < 3; ++tn) bf[tn] = cat8(trread(bb + boff + k2 * 16 * 192 + tn * 64), trread(bb + boff + k2 * 16 * 192 + 4 * 192 + tn * 64));
#pragma unroll
    for (int tm = 0; tm < 2; ++tm)
#pragma unroll
      for (int tn = 0; tn < 3; ++tn) acc[tm][tn] = mfma32(bf[tn], af[tm], acc[tm][tn]);
  }
}
template <int... I> DI void ffn1_all(iseq<I...>, f32x16 (&acc)[2][3], const XLoad& xl, rsrc_t rw, const unsigned (&bvoff)[3], unsigned bldst, lds_cptr lds, const int (&aoff)[2], int boff) {
  (ffn1_step<I>(acc, xl, rw, bvoff, bldst, lds, aoff, boff), ...);
}

DI void role_g(const void* x, const void* wqkv, const void* wo, const void* w1, const void* w2, const void* gw, char* ws, void* out, char* ldsg, int b, u64 gen) {
  __builtin_amdgcn_s_setprio(PRIO_G);
  const int tid = threadIdx.x, lane = tid & 63, w = rfl(tid >> 6), r32 = lane & 31, h = lane >> 5;
  const int j8 = b & 7, bi = (b - 256) >> 3, rg = j8 & 3, cg = j8 >> 2;
  const unsigned lds0 = (unsigned)(uintptr_t)ldsg;
  const lds_cptr lds = (lds_cptr)ldsg; const lds_ptr ldw = (lds_ptr)ldsg;
  const rsrc_t rws = mkrs(ws, WS_END);
  {
    const int rb = rg * 4 + (bi & 3), cb = cg * 8 + (bi >> 2), wm = w >> 1, kh = w & 1;
    XLoad xl; xl.init(x, rb, w, lane, lds0);
    const rsrc_t rw = mkrs(w1, D_ * F_ * 2);
    unsigned bvoff[3];
#pragma unroll
    for (int i = 0; i < 3; ++i) { const int idx = (w + 4 * i) * 64 + lane, k = idx / 12, pc = idx - k * 12; bvoff[i] = k * 3072 + cb * 192 + pc * 16; }
    const unsigned bldst = lds0 + 49152 + w * 1024;
    xl.issue(0, 0);
#pragma unroll
    for (int i = 0; i < 3; ++i) dma16(rw, bvoff[i], 0, bldst + i * 4096);
    xl.issue(1, 1);
    {
      const int row = rb * 128 + 8 * cb + 2 * w + h;
      float s = 0.f;
#pragma unroll
      for (int i = 0; i < 3; ++i) { const int c = r32 + 32 * i;
        if (c < 80) { const bf16x8 a = *(const bf16x8*)((const char*)x + row * 1280 + c * 16), g = *(const bf16x8*)((const char*)gw + c * 16);
#pragma unroll
          for (int e = 0; e < 8; ++e) s += (float)a[e] * (float)g[e]; } }
#pragma unroll
      for (int d = 1; d < 32; d <<= 1) s += __shfl_xor(s, d);
      if (r32 == 0) __hip_atomic_store((GAS unsigned*)(ws + WS_GATE + row * 4), __float_as_uint(1.f / (1.f + __expf(-s))), RLX_AGENT);
    }
    int aoff[2];
#pragma unroll
    for (int k2 = 0; k2 < 2; ++k2) aoff[k2] = (wm * 64 + r32) * 128 + (((2 * (2 * kh + k2) + h) ^ ((r32 >> 1) & 7)) << 4);
    const int boff = (kh * 32 + 8 * h + ((lane & 15) >> 2)) * 192 + (2 * ((lane >> 4) & 1) + ((lane & 3) >> 1)) * 16 + 8 * (lane & 1);
    f32x16 acc[2][3] = {{f32x16{}, f32x16{}, f32x16{}}, {f32x16{}, f32x16{}, f32x16{}}};
    ffn1_all(mkseq<10>::type{}, acc, xl, rw, bvoff, bldst, lds, aoff, boff);
    bar();
    TS(1);
    auto send = [&](const f32x16 (&a)[3]) {
#pragma unroll
      for (int tn = 0; tn < 3; ++tn)
#pragma unroll
        for (int q = 0; q < 4; ++q) *(LAS f32x4*)(ldw + w * 12288 + (tn * 4 + q) * 1024 + lane * 16) = f32x4{a[tn][4 * q], a[tn][4 * q + 1], a[tn][4 * q + 2], a[tn][4 * q + 3]};
    };
    const int rowtile = rb * 4 + wm * 2 + kh;
    auto fin = [&](const f32x16 (&a)[3]) {
#pragma unroll
      for (int tn = 0; tn < 3; ++tn) {
        unsigned pk[4][2];
#pragma unroll
        for (int q = 0; q < 4; ++q) {
          const f32x4 o = *(const LAS f32x4*)(lds + (w ^ 1) * 12288 + (tn * 4 + q) * 1024 + lane * 16);
          pk[q][0] = cvtpk(silu_f(a[tn][4 * q] + o[0]), silu_f(a[tn][4 * q + 1] + o[1])); pk[q][1] = cvtpk(silu_f(a[tn][4 * q + 2] + o[2]), silu_f(a[tn][4 * q + 3] + o[3])); }
#pragma unroll
        for (int q = 0; q < 4; q += 2) {
          auto r0 = __builtin_amdgcn_permlane32_swap(pk[q][0], pk[q + 1][0], false, false);
          auto r1 = __builtin_amdgcn_permlane32_swap(pk[q][1], pk[q + 1][1], false, false);
          st16_sc1(rws, WS_H + (rowtile * 192 + cb * 12 + tn * 4 + q + h) * 512 + r32 * 16, u32x4{r0[0], r1[0], r0[1], r1[1]});
        }
      }
    };
    if (kh == 0) send(acc[1]); else send(acc[0]);
    bar();
    if (kh == 0) fin(acc[0]); else fin(acc[1]);
    publish(ws, WS_FH + rb * 128 + cb * 8, gen);
    TS(2);
  }
  const int r64 = rg * 8 + (bi & 7), c80 = cg * 4 + (bi >> 3), rb = r64 >> 1;
  const unsigned ring = lds0 + w * 20480;
  const lds_cptr lring = lds + w * 20480;
  unsigned bv[5];
#pragma unroll
  for (int j = 0; j < 5; ++j) { const int idx = j * 64 + lane, k = idx / 10, pc = idx - k * 10; bv[j] = k * 1280 + c80 * 160 + pc * 16; }
  const int boff = (8 * (lane >> 4) + ((lane & 15) >> 2)) * 160 + 8 * (lane & 3);
  const unsigned avoff = (lane >> 4) * 512 + (lane & 15) * 16;
  f32x4 acc[4][5]; unsigned bpk[5][2];
  auto issueB = [&](rsrc_t rs, int k0, int i) {
#pragma unroll
    for (int j = 0; j < 5; ++j) dma16(rs, bv[j], (k0 + i * 32) * 1280, ring + (i & 3) * 5120 + j * 1024);
  };
  auto compute = [&](int i, const u32x4 (&af)[4]) {
    bf16x8 bf[5];
#pragma unroll
    for (int nt = 0; nt < 5; ++nt) bf[nt] = cat8(trread(lring + (i & 3) * 5120 + boff + nt * 32), trread(lring + (i & 3) * 5120 + boff + 4 * 160 + nt * 32));
#pragma unroll
    for (int mt = 0; mt < 4; ++mt)
#pragma unroll
      for (int nt = 0; nt < 5; ++nt) acc[mt][nt] = mfma16(bf[nt], __builtin_bit_cast(bf16x8, af[mt]), acc[mt][nt]);
  };
  auto reduce_scatter = [&](f32x4 (&res)[5]) {
    bar();
#pragma unroll
    for (int mt = 0; mt < 4; ++mt)
#pragma unroll
      for (int nt = 0; nt < 5; ++nt) *(LAS f32x4*)(ldw + ((mt * 4 + w) * 5 + nt) * 1024 + lane * 16) = acc[mt][nt];
    bar();
#pragma unroll
    for (int nt = 0; nt < 5; ++nt) {
      f32x4 s = *(const LAS f32x4*)(lds + ((w * 4) * 5 + nt) * 1024 + lane * 16);
#pragma unroll
      for (int sl = 1; sl < 4; ++sl) s += *(const LAS f32x4*)(lds + ((w * 4 + sl) * 5 + nt) * 1024 + lane * 16);
      res[nt] = s;
    }
    bar();
  };
  {
    f32x4 bsum[5];
    const rsrc_t r2 = mkrs(w2, F_ * D_ * 2);
    const int k0 = w * 384;
    bar();
    issueB(r2, k0, 0); issueB(r2, k0, 1); issueB(r2, k0, 2);
    wait_line(ws, WS_FH + rb * 128, gen, lane, 0xffffu);
    TS(3);
#pragma unroll
    for (int mt = 0; mt < 4; ++mt)
#pragma unroll
      for (int nt = 0; nt < 5; ++nt) acc[mt][nt] = f32x4{};
    u32x4 af[4][4];
    const unsigned as0 = WS_H + (r64 * 2 * 192 + w * 48) * 512, as1 = as0 + 192 * 512;
#define LDA(set, i) do { const unsigned o_ = (i) * 2048; LD16(af[set][0], rws, avoff, as0 + o_, 0); LD16(af[set][1], rws, avoff, as0 + o_, 256); LD16(af[set][2], rws, avoff, as1 + o_, 0); LD16(af[set][3], rws, avoff, as1 + o_, 256); } while (0)
#define WAITA(N, set) asm volatile("s_waitcnt vmcnt(" #N ") lgkmcnt(0)" : "+v"(af[set][0]), "+v"(af[set][1]), "+v"(af[set][2]), "+v"(af[set][3]) :: "memory")
    LDA(0, 0); LDA(1, 1); LDA(2, 2);
#pragma unroll
    for (int i = 0; i < 12; ++i) {
      if (i == 0) WAITA(8, 0);
      else if (i == 1) WAITA(13, 1);
      else if (i <= 9) { if ((i & 3) == 0) WAITA(18, 0); else if ((i & 3) == 1) WAITA(18, 1); else if ((i & 3) == 2) WAITA(18, 2); else WAITA(18, 3); }
      else if (i == 10) WAITA(9, 2);
      else WAITA(0, 3);
      if (i + 3 < 12) { issueB(r2, k0, i + 3); if (((i + 3) & 3) == 0) LDA(0, i + 3); else if (((i + 3) & 3) == 1) LDA(1, i + 3); else if (((i + 3) & 3) == 2) LDA(2, i + 3); else LDA(3, i + 3); }
      compute(i, af[i & 3]);
    }
    reduce_scatter(bsum);
#pragma unroll
    for (int nt = 0; nt < 5; ++nt) { bpk[nt][0] = cvtpk(bsum[nt][0], bsum[nt][1]); bpk[nt][1] = cvtpk(bsum[nt][2], bsum[nt][3]); }
    TS(4);
  }
  if (b == 511) {
    for (int l = 0; l < 16; ++l) { wait_line(ws, WS_FKV + l * 512, gen, lane, 0xffffu); wait_line(ws, WS_FH + l * 128, gen, lane, 0xffffu); }
    if (tid == 0) __hip_atomic_store((gu64*)(ws + WS_GEN), gen, RLX_AGENT);
  }
  {
    const rsrc_t ro = mkrs(wo, D_ * D_ * 2);
    const int k0 = w * 160;
    issueB(ro, k0, 0); issueB(ro, k0, 1); issueB(ro, k0, 2); issueB(ro, k0, 3);
    const float g = __uint_as_float(__hip_atomic_load((GAS unsigned*)(ws + WS_GATE + (r64 * 64 + w * 16 + (lane & 15)) * 4), RLX_AGENT));
#pragma unroll
    for (int mt = 0; mt < 4; ++mt)
#pragma unroll
      for (int nt = 0; nt < 5; ++nt) acc[mt][nt] = f32x4{};
    wait_line(ws, WS_FO + rb * 128, gen, lane, 0xffffu);
    TS(5);
    u32x4 af[5][4];
    const unsigned as0 = WS_O + (r64 * 2 * 80 + w * 20) * 512, as1 = as0 + 80 * 512;
    LDA(0, 0); LDA(1, 1); LDA(2, 2); LDA(3, 3); LDA(4, 4);
    WAITA(16, 0); compute(0, af[0]);
    asm volatile("s_waitcnt lgkmcnt(0)" ::: "memory");
    issueB(ro, k0, 4);
    WAITA(17, 1); compute(1, af[1]);
    WAITA(13, 2); compute(2, af[2]);
    WAITA(9, 3); compute(3, af[3]);
    WAITA(0, 4); compute(4, af[4]);
    f32x4 asum[5];
    reduce_scatter(asum);
    const int row = r64 * 64 + w * 16 + (lane & 15);
#pragma unroll
    for (int nt = 0; nt < 5; ++nt) {
      const f32x4 bb = {__uint_as_float(bpk[nt][0] << 16), __uint_as_float(bpk[nt][0] & 0xffff0000u), __uint_as_float(bpk[nt][1] << 16), __uint_as_float(bpk[nt][1] & 0xffff0000u)};
      const f32x4 v = g * asum[nt] + (1.f - g) * bb;
      *(u32x2*)((char*)out + row * 1280 + (c80 * 80 + nt * 16 + 4 * (lane >> 4)) * 2) = u32x2{cvtpk(v[0], v[1]), cvtpk(v[2], v[3])};
    }
    TS(6);
  }
}

__global__ __launch_bounds__(256, 2) void k_fused(const void* x, const void* wqkv, const void* wo, const void* w1, const void* w2, const void* gw, char* ws, void* out) {
  __shared__ __attribute__((aligned(16))) char lds[LDS_BYTES];
  const u64 gen = __hip_atomic_load((gu64*)(ws + WS_GEN), RLX_AGENT) + 1;
  const unsigned glo = rfl((unsigned)gen), ghi = rfl((unsigned)(gen >> 32));
  const u64 g = ((u64)ghi << 32) | glo;
  const int b = blockIdx.x;
  TS(0);
  if (b < 256) role_a(x, wqkv, ws, lds, b, g);
  else role_g(x, wqkv, wo, w1, w2, gw, ws, out, lds, b, g);
}

extern "C" void kernel_launch(void* const* d_in, const int* in_sizes, int n_in, void* d_out, int out_size, void* d_ws, size_t ws_size, hipStream_t stream) {
  k_fused<<<512, 256, 0, stream>>>(d_in[0], d_in[1], d_in[2], d_in[4], d_in[5], d_in[6], (char*)d_ws, d_out);
}
```

```cpp
#include <hip/hip_runtime.h>
#include <hip/hip_bf16.h>
#include <stdint.h>

typedef __bf16 bf16x8 __attribute__((ext_vector_type(8)));
typedef __bf16 bf16x2 __attribute__((ext_vector_type(2)));
typedef short s16x4 __attribute__((ext_vector_type(4)));
typedef float f32x16 __attribute__((ext_vector_type(16)));
typedef float f32x4 __attribute__((ext_vector_type(4)));
typedef float f32x2 __attribute__((ext_vector_type(2)));
typedef unsigned u32x4 __attribute__((ext_vector_type(4)));
typedef unsigned u32x2 __attribute__((ext_vector_type(2)));
typedef unsigned long long u64;
#define DI __device__ __forceinline__
#define SC1L " sc1"
#define AUXL 16
#define LAS __attribute__((address_space(3)))
#define GAS __attribute__((address_space(1)))
typedef LAS const char* lds_cptr;
typedef LAS char* lds_ptr;
typedef GAS u64 gu64;
typedef __amdgpu_buffer_rsrc_t rsrc_t;
#define RLX_AGENT __ATOMIC_RELAXED, __HIP_MEMORY_SCOPE_AGENT

constexpr int S_ = 2048, D_ = 640, F_ = 1536;
constexpr float QSCALE = 0.22360679774997896f * 1.4426950408889634f;
constexpr float LOG2E = 1.4426950408889634f;

constexpr unsigned WS_GEN = 0;
constexpr unsigned WS_ERR = 64;
constexpr unsigned WS_FQKV = 4096;
constexpr unsigned WS_FH = 57344;
constexpr unsigned WS_FO = 49152;
constexpr unsigned WS_HINT = 32768;
constexpr unsigned WS_FG = 34816;
constexpr unsigned WS_FB = 36864;
constexpr unsigned WS_GATE = 40960;
constexpr unsigned KVCH = 5120;
constexpr unsigned KV_KT = 2048, KV_V = 2560;
constexpr unsigned WS_KV = 65536;
constexpr unsigned QCH = 2560;
constexpr unsigned WS_Q = WS_KV + 32 * 32 * KVCH;
constexpr unsigned WS_H = WS_Q + 32 * 32 * QCH;
constexpr unsigned WS_O = WS_H + S_ * F_ * 2;
constexpr unsigned WS_PB = WS_O + S_ * D_ * 2;
constexpr unsigned WS_END = WS_PB + 4 * S_ * D_ * 2;
constexpr int LDS_BYTES = 81920;

DI unsigned cvtpk(float lo, float hi) { f32x2 v = {lo, hi}; bf16x2 b = __builtin_convertvector(v, bf16x2); return __builtin_bit_cast(unsigned, b); }
DI f32x16 mfma32(bf16x8 a, bf16x8 b, f32x16 c) { return __builtin_amdgcn_mfma_f32_32x32x16_bf16(a, b, c, 0, 0, 0); }
DI f32x16 mfma32k8(s16x4 a, s16x4 b, f32x16 c) { return __builtin_amdgcn_mfma_f32_32x32x8bf16_1k(a, b, c, 0, 0, 0); }
DI f32x4 mfma16(bf16x8 a, bf16x8 b, f32x4 c) { return __builtin_amdgcn_mfma_f32_16x16x32_bf16(a, b, c, 0, 0, 0); }
DI rsrc_t mkrs(const void* p, unsigned bytes) { return __builtin_amdgcn_make_buffer_rsrc((void*)p, (short)0, (int)bytes, 0x00020000); }
DI void dma16(rsrc_t rs, unsigned voff, unsigned soff, unsigned ldsdst) {
  asm volatile("s_mov_b32 m0, %0\n\ts_nop 0\n\tbuffer_load_dwordx4 %1, %2, %3 offen lds" :: "s"(ldsdst), "v"(voff), "s"(rs), "s"(soff) : "memory", "m0");
}
DI void dma16_sc1(rsrc_t rs, unsigned voff, unsigned soff, unsigned ldsdst) {
  asm volatile("s_mov_b32 m0, %0\n\ts_nop 0\n\tbuffer_load_dwordx4 %1, %2, %3 offen" SC1L " lds" :: "s"(ldsdst), "v"(voff), "s"(rs), "s"(soff) : "memory", "m0");
}
DI void st16_sc1(rsrc_t rs, unsigned off, u32x4 v) { __builtin_amdgcn_raw_buffer_store_b128(v, rs, (int)off, 0, 16); }
typedef short v4i16_t __attribute__((ext_vector_type(4)));
DI s16x4 trread(lds_cptr p) { return __builtin_bit_cast(s16x4, __builtin_amdgcn_ds_read_tr16_b64_v4i16((LAS v4i16_t*)p)); }
DI bf16x8 cat8(s16x4 lo, s16x4 hi) { return __builtin_bit_cast(bf16x8, __builtin_shufflevector(lo, hi, 0, 1, 2, 3, 4, 5, 6, 7)); }
template <int N> DI void wait_vm() { asm volatile("s_waitcnt vmcnt(%0)" :: "i"(N) : "memory"); }
DI void bar() { asm volatile("s_waitcnt lgkmcnt(0)\n\ts_barrier" ::: "memory"); }
DI unsigned rfl(unsigned x) { return __builtin_amdgcn_readfirstlane(x); }
#define SBAR() __builtin_amdgcn_sched_barrier(0)
#define PIN(x) asm volatile("" : "+v"(x))
constexpr unsigned ONE2 = 0x3F803F80u;
constexpr unsigned SPIN_CAP = 40000;

DI unsigned poll_line(const char* ws, unsigned off, u64 gen, int lane) {
  const u64 v = __hip_atomic_load((gu64*)(ws + off + (lane & 15) * 8), RLX_AGENT);
  return (unsigned)__ballot(v == gen) & 0xffffu;
}
DI unsigned wait_line(const char* ws, unsigned off, u64 gen, int lane, unsigned needmask) {
  unsigned m = poll_line(ws, off, gen, lane);
  for (unsigned spins = 0; (m & needmask) != needmask; ++spins) {
    if (spins > SPIN_CAP) { if (lane == 0) __hip_atomic_store((gu64*)(ws + WS_ERR), gen, RLX_AGENT); return 0xffffu; }
    __builtin_amdgcn_s_sleep(6);
    m = poll_line(ws, off, gen, lane);
  }
  return m;
}
DI void wait_line_blk(const char* ws, unsigned off, u64 gen, unsigned needmask) {
  if (threadIdx.x < 64) wait_line(ws, off, gen, threadIdx.x, needmask);
  bar();
}
DI void publish(char* ws, unsigned off, u64 gen) {
  wait_vm<0>(); bar();
  if (threadIdx.x == 0) __hip_atomic_store((gu64*)(ws + off), gen, RLX_AGENT);
}

DI unsigned rotk(int i, int rot, int n) { const int k = i + rot; return k >= n ? k - n : k; }
struct XLoad {
  rsrc_t rs; unsigned voff, sbase, ldst; int rot;
  DI void init(const void* x, int rb, int w, int lane, unsigned lds0) {
    rs = mkrs(x, S_ * D_ * 2);
    voff = (8 * w + (lane >> 3)) * 1280 + (((lane & 7) ^ ((4 * (w & 1) + (lane >> 4)) & 7)) << 4);
    sbase = rb * 128 * 1280; ldst = lds0 + w * 1024;
  }
  DI void issue1(int stage, int slot, int ii) const { dma16(rs, voff, sbase + ii * 32 * 1280 + rotk(stage, rot, 10) * 128, ldst + slot * 16384 + ii * 4096); }
  DI void issue(int stage, int slot) const {
#pragma unroll
    for (int ii = 0; ii < 4; ++ii) dma16(rs, voff, sbase + ii * 32 * 1280 + rotk(stage, rot, 10) * 128, ldst + slot * 16384 + ii * 4096);
  }
};

constexpr int LX = 0, LQ = 32768, LF = 57344;
constexpr int mg_wait(int tgt) {
  int pos = 0, pX[10] = {}, pQ[20] = {}, pF[10] = {};
  pos += 4; pX[0] = pos; pos += 2; pQ[0] = pos; pos += 3; pF[0] = pos; pos += 2; pQ[1] = pos; pos += 4; pX[1] = pos; pos += 3; pF[1] = pos; pos += 2; pQ[2] = pos;
  for (int h = 0; h < tgt - 1; ++h) {
    if (h + 3 < 20) { pos += 2; pQ[h + 3] = pos; }
    if ((h & 1) && (h + 3) / 2 < 10) { pos += 4; pX[(h + 3) / 2] = pos; pos += 3; pF[(h + 3) / 2] = pos; }
  }
  int need = pQ[tgt]; if (pX[tgt / 2] > need) need = pX[tgt / 2]; if (pF[tgt / 2] > need) need = pF[tgt / 2];
  return pos - need;
}
struct KFrag { bf16x8 m; s16x4 t; };
struct VFrag { bf16x8 a, b; };

struct MgCtx { XLoad xl; rsrc_t rq, rf; unsigned qv, qd, fv[3], fd; int aoff[4], asoff[4], boff[2], foff, f1off; lds_cptr lds; };
DI void mg_issueQ(const MgCtx& c, int hs, int i) { dma16(c.rq, c.qv, rotk(hs, 2 * c.xl.rot, 20) * 32 * 3840 + i * 16 * 3840, c.qd + (hs % 3) * 8192 + i * 4096); }
DI void mg_issueF(const MgCtx& c, int st, int i) { dma16(c.rf, c.fv[i], rotk(st, c.xl.rot, 10) * 64 * 3072, c.fd + (st & 1) * 12288 + i * 4096); }
struct GRaw { bf16x8 a[2][2], as[2]; s16x4 b[2][2][2], f[2][2][2]; };
template <int T, int I> DI void mg_ld(GRaw& f, const MgCtx& c, lds_cptr q0, lds_cptr q1) {
  constexpr int k2 = I / 11, j = I % 11, ks = 2 * (T & 1) + k2;
  const lds_cptr xb = c.lds + LX + ((T / 2) & 1) * 16384, fb = c.lds + LF + ((T / 2) & 1) * 12288 + ((T & 1) * 32 + k2 * 16) * 192;
  if constexpr (j == 0) f.a[k2][0] = *(const LAS bf16x8*)(xb + c.aoff[ks]);
  else if constexpr (j == 1) f.b[k2][0][0] = trread(q0 + k2 * 16 * 256);
  else if constexpr (j == 2) f.b[k2][0][1] = trread(q0 + k2 * 16 * 256 + 4 * 256);
  else if constexpr (j == 3) f.b[k2][1][0] = trread(q1 + k2 * 16 * 256);
  else if constexpr (j == 4) f.b[k2][1][1] = trread(q1 + k2 * 16 * 256 + 4 * 256);
  else if constexpr (j == 5) f.a[k2][1] = *(const LAS bf16x8*)(xb + c.aoff[ks] + 4096);
  else if constexpr (j == 6) f.f[k2][0][0] = trread(fb + c.foff);
  else if constexpr (j == 7) f.f[k2][0][1] = trread(fb + c.foff + 4 * 192);
  else if constexpr (j == 8) f.f[k2][1][0] = trread(fb + c.f1off);
  else if constexpr (j == 9) f.f[k2][1][1] = trread(fb + c.f1off + 4 * 192);
  else f.as[k2] = *(const LAS bf16x8*)(xb + c.asoff[ks]);
}
template <bool SW> DI f32x16 mmq(bf16x8 b, bf16x8 a, f32x16 c) { return SW ? mfma32(b, a, c) : mfma32(a, b, c); }
template <bool SW, int HS, bool TAIL> DI void mg_step(f32x16 (&acc)[2][2], f32x16 (&accf)[3], GRaw (&fr)[2], const MgCtx& c, int hs, int qs) {
  constexpr bool NX = !TAIL || HS + 1 < 20, DQ = !TAIL || HS + 3 < 20, DX = (HS & 1) && (!TAIL || (HS + 3) / 2 < 10);
  if constexpr (NX) { wait_vm<TAIL ? mg_wait(HS + 1) : ((HS & 1) ? 2 : 9)>(); bar(); }
  const GRaw& f = fr[HS & 1]; GRaw& n = fr[(HS + 1) & 1];
  const int qn = qs == 2 ? 0 : qs + 1;
  const lds_cptr q0 = c.lds + LQ + c.boff[0] + qn * 8192, q1 = c.lds + LQ + c.boff[1] + qn * 8192;
  const int s2 = (hs + 3) >> 1; constexpr int SL = ((HS + 3) / 2) & 1;
  const unsigned qso = rotk(hs + 3, 2 * c.xl.rot, 20) * 32 * 3840, qd = c.qd + qs * 8192;
  const unsigned st = rotk(s2, c.xl.rot, 10);
#define LD2(i) do { SBAR(); if constexpr (NX) { mg_ld<HS + 1, i>(n, c, q0, q1); mg_ld<HS + 1, i + 1>(n, c, q0, q1); } SBAR(); } while (0)
#define DQ_(i) do { if constexpr (DQ) dma16(c.rq, c.qv, qso + i * 16 * 3840, qd + i * 4096); SBAR(); } while (0)
#define DX_(i) do { if constexpr (DX) dma16(c.xl.rs, c.xl.voff, c.xl.sbase + i * 32 * 1280 + st * 128, c.xl.ldst + SL * 16384 + i * 4096); SBAR(); } while (0)
#define DF_(i) do { if constexpr (DX) dma16(c.rf, c.fv[i], st * 64 * 3072, c.fd + SL * 12288 + i * 4096); SBAR(); } while (0)
#define BQ(k, t) cat8(f.b[k][t][0], f.b[k][t][1])
#define BF(k, t) cat8(f.f[k][t][0], f.f[k][t][1])
  SBAR();
  acc[0][0] = mmq<SW>(BQ(0, 0), f.a[0][0], acc[0][0]); LD2(0);
  acc[0][1] = mmq<SW>(BQ(0, 1), f.a[0][0], acc[0][1]); LD2(2); DQ_(0);
  acc[1][0] = mmq<SW>(BQ(0, 0), f.a[0][1], acc[1][0]); LD2(4);
  acc[1][1] = mmq<SW>(BQ(0, 1), f.a[0][1], acc[1][1]); LD2(6); DQ_(1);
  accf[0] = mfma32(BF(0, 0), f.a[0][0], accf[0]); LD2(8);
  accf[1] = mfma32(BF(0, 0), f.a[0][1], accf[1]); LD2(10); DX_(0);
  accf[2] = mfma32(BF(0, 1), f.as[0], accf[2]); LD2(12);
  acc[0][0] = mmq<SW>(BQ(1, 0), f.a[1][0], acc[0][0]); LD2(14); DX_(1);
  acc[0][1] = mmq<SW>(BQ(1, 1), f.a[1][0], acc[0][1]); LD2(16);
  acc[1][0] = mmq<SW>(BQ(1, 0), f.a[1][1], acc[1][0]); LD2(18); DX_(2);
  acc[1][1] = mmq<SW>(BQ(1, 1), f.a[1][1], acc[1][1]); LD2(20); DX_(3);
  accf[0] = mfma32(BF(1, 0), f.a[1][0], accf[0]); SBAR(); DF_(0);
  accf[1] = mfma32(BF(1, 0), f.a[1][1], accf[1]); SBAR(); DF_(1);
  accf[2] = mfma32(BF(1, 1), f.as[1], accf[2]); SBAR(); DF_(2);
}
template <int... I> struct iseq {};
template <int N, int... I> struct mkseq : mkseq<N - 1, N - 1, I...> {};
template <int... I> struct mkseq<0, I...> { typedef iseq<I...> type; };
template <int... J> DI void mg_ld_all0(iseq<J...>, GRaw& f, const MgCtx& c) { (mg_ld<0, J>(f, c, c.lds + LQ + c.boff[0], c.lds + LQ + c.boff[1]), ...); }
template <bool SW> DI void mg_all(f32x16 (&acc)[2][2], f32x16 (&accf)[3], const MgCtx& c) {
  GRaw fr[2];
  wait_vm<mg_wait(0)>(); bar();
  mg_ld_all0(mkseq<22>::type{}, fr[0], c);
  int qs = 0;
  for (int hs = 0; hs < 16; hs += 4) {
    mg_step<SW, 0, false>(acc, accf, fr, c, hs, qs); qs = qs == 2 ? 0 : qs + 1;
    mg_step<SW, 1, false>(acc, accf, fr, c, hs + 1, qs); qs = qs == 2 ? 0 : qs + 1;
    mg_step<SW, 2, false>(acc, accf, fr, c, hs + 2, qs); qs = qs == 2 ? 0 : qs + 1;
    mg_step<SW, 3, false>(acc, accf, fr, c, hs + 3, qs); qs = qs == 2 ? 0 : qs + 1;
  }
  mg_step<SW, 16, true>(acc, accf, fr, c, 16, 1); mg_step<SW, 17, true>(acc, accf, fr, c, 17, 2); mg_step<SW, 18, true>(acc, accf, fr, c, 18, 0); mg_step<SW, 19, true>(acc, accf, fr, c, 19, 1);
}

constexpr int ASLOT = KVCH + 48;
constexpr int ARING = 3 * ASLOT;
constexpr int XCH = 62080;

template <int IMM> DI void dma16_sc1_imm(rsrc_t rs, unsigned voff, unsigned soff, unsigned ldsdst) {
  asm volatile("s_mov_b32 m0, %0\n\ts_nop 0\n\tbuffer_load_dwordx4 %1, %2, %3 offen offset:%4" SC1L " lds" :: "s"(ldsdst), "v"(voff), "s"(rs), "s"(soff), "i"(IMM) : "memory", "m0");
}

DI void st8_sc1(rsrc_t rs, unsigned off, u32x2 v) { __builtin_amdgcn_raw_buffer_store_b64(v, rs, (int)off, 0, 16); }
template <int TN, int J> DI void qkv_store(const f32x16 (&acc)[2][2], rsrc_t rws, const unsigned (&so)[8], unsigned vA, unsigned vT, unsigned tt, float s) {
  const unsigned o = so[TN * 4 + J];
  u32x2 v[2];
#pragma unroll
  for (int tm = 0; tm < 2; ++tm) { const f32x16& a = acc[tm][TN]; v[tm] = u32x2{cvtpk(a[4 * J] * s, a[4 * J + 1] * s), cvtpk(a[4 * J + 2] * s, a[4 * J + 3] * s)}; }
  if (!(o & 1)) {
    auto r0 = __builtin_amdgcn_permlane32_swap(v[0].x, v[1].x, false, false);
    auto r1 = __builtin_amdgcn_permlane32_swap(v[0].y, v[1].y, false, false);
    const u32x4 d = {r0[0], r1[0], r0[1], r1[1]};
    asm volatile("s_nop 1\n\tbuffer_store_dwordx4 %0, %1, %2, %3 offen sc1\n\ts_nop 1" :: "v"(d), "v"(vA), "s"(rws), "s"(o) : "memory");
  } else {
    __builtin_amdgcn_raw_buffer_store_b64(v[0], rws, (int)vT, (int)(o - 1), 16);
    __builtin_amdgcn_raw_buffer_store_b64(v[1], rws, (int)vT, (int)(o - 1 + tt), 16);
  }
}

DI float silu_f(float v) { return v * __builtin_amdgcn_rcpf(1.f + __builtin_amdgcn_exp2f(-LOG2E * v)); }
DI void role_a(const void* x, const void* wqkv, const void* w1, char* ws, char* ldsg, int b) {
  const u64 gv = __hip_atomic_load((gu64*)(ws + WS_GEN), RLX_AGENT);
  u64 gen = 0;
  const int tid = threadIdx.x, lane = tid & 63, w = rfl(tid >> 6), r32 = lane & 31, h = lane >> 5;
  const int j8 = b & 7, bi = b >> 3, rb = bi & 15, pr = 2 * j8 + (bi >> 4);
  const unsigned lds0 = (unsigned)(uintptr_t)ldsg;
  const lds_cptr lds = (lds_cptr)ldsg; const lds_ptr ldw = (lds_ptr)ldsg;
  const rsrc_t rws = mkrs(ws, WS_END);
  const int hh = w >> 1, kh = w & 1, head = 2 * pr + hh;
  const int ring = w * ARING;
  __builtin_amdgcn_s_setprio(3);
  const int qrb = (j8 & 1) * 8 + (bi & 7), cb = (j8 >> 1) * 4 + (bi >> 3);
  {
    const int wm = w >> 1, wn = w & 1, rb = qrb, cq = cb < 15 ? cb : 14;
    MgCtx c; c.lds = lds;
    c.xl.init(x, rb, w, lane, lds0 + LX);
    c.xl.rot = ((bi & 7) + 3 * (bi >> 3)) % 10;
    c.rq = mkrs(wqkv, D_ * 1920 * 2); c.rf = mkrs(w1, D_ * F_ * 2);
    c.qv = (4 * w + (lane >> 4)) * 3840 + cq * 256 + ((lane & 15) ^ (((lane >> 4) & 3) << 2)) * 16; c.qd = lds0 + LQ + w * 1024;
#pragma unroll
    for (int i = 0; i < 3; ++i) { const int idx = (w + 4 * i) * 64 + lane, k = idx / 12, pc = idx - k * 12; c.fv[i] = k * 3072 + cb * 192 + pc * 16; }
    c.fd = lds0 + LF + w * 1024;
    c.xl.issue(0, 0); mg_issueQ(c, 0, 0); mg_issueQ(c, 0, 1);
#pragma unroll
    for (int i = 0; i < 3; ++i) mg_issueF(c, 0, i);
    mg_issueQ(c, 1, 0); mg_issueQ(c, 1, 1);
    c.xl.issue(1, 1);
#pragma unroll
    for (int i = 0; i < 3; ++i) mg_issueF(c, 1, i);
    mg_issueQ(c, 2, 0); mg_issueQ(c, 2, 1);
#pragma unroll
    for (int ks = 0; ks < 4; ++ks) { c.aoff[ks] = (wm * 64 + r32) * 128 + (((2 * ks + h) ^ ((r32 >> 1) & 7)) << 4); c.asoff[ks] = c.aoff[ks] + wn * 4096; }
    const int blk = (lane >> 4) & 1, q = (lane & 15) >> 2, pp = lane & 3;
#pragma unroll
    for (int tn = 0; tn < 2; ++tn) c.boff[tn] = (8 * h + q) * 256 + (((wn * 8 + tn * 4 + 2 * blk + (pp >> 1)) ^ (q << 2)) << 4) + 8 * (pp & 1);
    c.f1off = (8 * h + q) * 192 + 64 + blk * 32 + pp * 8;
    c.foff = c.f1off - 64 + wn * 128;
    f32x16 acc[2][2] = {{f32x16{}, f32x16{}}, {f32x16{}, f32x16{}}}, accf[3] = {f32x16{}, f32x16{}, f32x16{}};
    const int part = cq / 5;
    const unsigned hcoff = rb * 2 + wm;
    unsigned so[8]; const unsigned hs = 32 * (part == 0 ? QCH : KVCH); const float sc = part == 0 ? QSCALE : 1.f;
    {
      const int g00 = (cq - part * 5) * 32 + wn * 16;
      const unsigned ub = part == 0 ? WS_Q + hcoff * QCH : WS_KV + hcoff * KVCH;
#pragma unroll
      for (int i = 0; i < 8; ++i) {
        const int g0 = g00 + 2 * i, pr_ = g0 / 10, R = g0 - pr_ * 10;
        const unsigned pb = ub + pr_ * 2 * hs;
        so[i] = R == 4 ? pb + 2048 + 1 : pb + (R > 4 ? hs : 0u) + ((R > 4 ? R - 6 : R) >> 1) * 512;
        asm volatile("" : "+s"(so[i]));
      }
    }
    if (part < 2) {
      mg_all<true>(acc, accf, c);

      if (cb < 15) {
        const unsigned vA = h * 1024 + r32 * 16, vT = h * hs + r32 * 8;
        qkv_store<0, 0>(acc, rws, so, vA, vT, 256, sc); qkv_store<0, 1>(acc, rws, so, vA, vT, 256, sc); qkv_store<0, 2>(acc, rws, so, vA, vT, 256, sc); qkv_store<0, 3>(acc, rws, so, vA, vT, 256, sc);
        qkv_store<1, 0>(acc, rws, so, vA, vT, 256, sc); qkv_store<1, 1>(acc, rws, so, vA, vT, 256, sc); qkv_store<1, 2>(acc, rws, so, vA, vT, 256, sc); qkv_store<1, 3>(acc, rws, so, vA, vT, 256, sc);
      }
    } else {
      mg_all<false>(acc, accf, c);

      if (cb < 15) {
#pragma unroll
        for (int tn = 0; tn < 2; ++tn) {
          const int n = (cb - 10) * 128 + wn * 64 + tn * 32 + r32, hd = n / 20, d = n - hd * 20, dp = (hd & 1) ? (d < 4 ? d + 16 : d - 4) : d;
          const unsigned vo = WS_KV + (hd * 32 + hcoff) * KVCH + KV_V + h * 320 + dp * 16;
#pragma unroll
          for (int tm = 0; tm < 2; ++tm)
#pragma unroll
            for (int s2 = 0; s2 < 2; ++s2) { const f32x16& a = acc[tm][tn];
              st16_sc1(rws, vo + (tm * 2 + s2) * 640, u32x4{cvtpk(a[8 * s2], a[8 * s2 + 1]), cvtpk(a[8 * s2 + 2], a[8 * s2 + 3]), cvtpk(a[8 * s2 + 4], a[8 * s2 + 5]), cvtpk(a[8 * s2 + 6], a[8 * s2 + 7])}); }
        }
      }
    }
    gen = (((u64)rfl((unsigned)(gv >> 32)) << 32) | rfl((unsigned)gv)) + 1;

    auto hstore = [&](const f32x16& a, int rowtile, int ct) {
      unsigned pk[4][2];
#pragma unroll
      for (int g = 0; g < 4; ++g) { pk[g][0] = cvtpk(silu_f(a[4 * g]), silu_f(a[4 * g + 1])); pk[g][1] = cvtpk(silu_f(a[4 * g + 2]), silu_f(a[4 * g + 3])); }
      u32x4 r[2];
#pragma unroll
      for (int g = 0; g < 4; g += 2) {
        auto r0 = __builtin_amdgcn_permlane32_swap(pk[g][0], pk[g + 1][0], false, false);
        auto r1 = __builtin_amdgcn_permlane32_swap(pk[g][1], pk[g + 1][1], false, false);
        r[g >> 1] = u32x4{r0[0], r1[0], r0[1], r1[1]};
      }
      return [=]() {
#pragma unroll
        for (int g = 0; g < 2; ++g) st16_sc1(rws, WS_H + (rowtile * 192 + cb * 12 + ct * 4 + 2 * g + h) * 512 + r32 * 16, r[g]); };
    };
    auto s0 = hstore(accf[0], rb * 4 + wm * 2, wn * 2);
    wait_vm<0>(); bar();
    if (tid == 0) { if (cb < 15) __hip_atomic_store((gu64*)(ws + WS_FQKV + cb * 128 + rb * 8), gen, RLX_AGENT); __hip_atomic_store((gu64*)(ws + WS_HINT + b * 8), gen, RLX_AGENT); }

    s0();
    hstore(accf[1], rb * 4 + wm * 2 + 1, wn * 2)();
    hstore(accf[2], rb * 4 + wm * 2 + wn, 1)();

    wait_vm<0>();
    if (lane == 0) __hip_atomic_store((gu64*)(ws + WS_FH + (qrb * 4 + (cb >> 2)) * 128 + ((cb & 3) * 4 + w) * 8), gen, RLX_AGENT);
  }
  if (tid < 12) { *(LAS u32x4*)(ldw + tid * ASLOT + KVCH) = u32x4{ONE2, 0x00003F80u, 0u, 0u}; *(LAS u32x4*)(ldw + tid * ASLOT + KVCH + 16) = u32x4{ONE2, ONE2, ONE2, ONE2}; *(LAS u32x4*)(ldw + tid * ASLOT + KVCH + 32) = u32x4{0u, 0u, 0u, 0u}; }
  __builtin_amdgcn_s_setprio(1);
  const int cA = (40 * pr) >> 7, cB = (40 * pr + 39) >> 7;
  const char* fptr = ws + WS_FQKV + ((lane < 32 ? 5 : 10) + ((lane & 16) ? cB : cA)) * 128 + (lane & 15) * 8;
  const char* qptr = ws + WS_FQKV + ((lane & 1) ? cB : cA) * 128 + rb * 8;
  unsigned ready = 0;
  auto chunk_of = [&](int n) { return (rb + n) & 15; };
  auto wait_mask = [&](unsigned kvbits, bool needq) {
    for (unsigned spins = 0;; ++spins) {
      const u64 m = __ballot(__hip_atomic_load((gu64*)fptr, RLX_AGENT) == gen);
      const bool qok = !needq || __all(__hip_atomic_load((gu64*)qptr, RLX_AGENT) == gen);
      ready = (unsigned)(m & (m >> 16) & (m >> 32) & (m >> 48)) & 0xffffu;
      if ((ready & kvbits) == kvbits && qok) return;
      if (spins > SPIN_CAP) { if (lane == 0) __hip_atomic_store((gu64*)(ws + WS_ERR), gen, RLX_AGENT); ready = 0xffffu; return; }
      __builtin_amdgcn_s_sleep(1);
    }
  };
  auto need = [&](int n) { const int ch = chunk_of(n); if (!((ready >> ch) & 1)) wait_mask(1u << ch, false); };
  int nextc = 0;
  auto pick = [&]() -> int {
    const int ch = (rb + nextc) & 15; ++nextc;
    if (__builtin_expect(!((ready >> ch) & 1), 0)) wait_mask(1u << ch, false);
    return ch;
  };
  const unsigned l16 = lane * 16;
#define DMA_PIECE(n, so, i) do { const unsigned g_ = WS_KV + (head * 32 + chunk_of(n) * 2 + kh) * KVCH + ((i) >> 2) * 4096; const unsigned d_ = lds0 + ring + (so) + ((i) >> 2) * 4096; \
    dma16_sc1_imm<((i) & 3) * 1024>(rws, l16, g_, d_); } while (0)
#define DMA_CH(ch, so) do { const unsigned g_ = WS_KV + (head * 32 + (ch) * 2 + kh) * KVCH, d_ = lds0 + ring + (so); \
    dma16_sc1_imm<0>(rws, l16, g_, d_); dma16_sc1_imm<1024>(rws, l16, g_, d_); dma16_sc1_imm<2048>(rws, l16, g_, d_); dma16_sc1_imm<3072>(rws, l16, g_, d_); dma16_sc1_imm<0>(rws, l16, g_ + 4096, d_ + 4096); } while (0)
#define DMA_ALL(n, so) do { DMA_PIECE(n, so, 0); DMA_PIECE(n, so, 1); DMA_PIECE(n, so, 2); DMA_PIECE(n, so, 3); DMA_PIECE(n, so, 4); } while (0)
  bar();
  wait_mask(1u << rb, true);

  bf16x8 qm[4]; s16x4 qa[4]; u32x2 qtl[4];
#pragma unroll
  for (int qt = 0; qt < 4; ++qt) {
    const unsigned qb = WS_Q + (head * 32 + rb * 2 + (qt >> 1)) * QCH;
    qm[qt] = __builtin_bit_cast(bf16x8, __builtin_amdgcn_raw_buffer_load_b128(rws, (int)(qb + (qt & 1) * 1024 + lane * 16), 0, AUXL));
    qtl[qt] = __builtin_bit_cast(u32x2, __builtin_amdgcn_raw_buffer_load_b64(rws, (int)(qb + 2048 + (qt & 1) * 256 + r32 * 8), 0, AUXL));
  }
  { const int c0 = pick(); DMA_CH(c0, 0); const int c1 = pick(); DMA_CH(c1, ASLOT); }

#pragma unroll
  for (int qt = 0; qt < 4; ++qt) if (h) qtl[qt] = u32x2{0u, 0u};
  const lds_cptr lk = lds + ring + lane * 16;
  lds_cptr lt[2], lv[2][2];
#pragma unroll
  for (int t = 0; t < 2; ++t) {
    lt[t] = lds + ring + (h ? (int)KVCH : (int)KV_KT + t * 256 + r32 * 8);
#pragma unroll
    for (int s2 = 0; s2 < 2; ++s2) lv[t][s2] = lds + ring + (r32 < 20 ? (int)KV_V + (t * 2 + s2) * 640 + h * 320 + r32 * 16 : (int)KVCH + (r32 == 20 ? 16 : 32));
  }
  auto lK = [&](int so, int t) { KFrag k; k.m = *(const LAS bf16x8*)(lk + so + t * 1024); k.t = *(const LAS s16x4*)(lt[t] + so); return k; };
  auto lV = [&](int so, int t) { VFrag v; v.a = *(const LAS bf16x8*)(lv[t][0] + so); v.b = *(const LAS bf16x8*)(lv[t][1] + so); return v; };
  wait_vm<5>();

  f32x16 o[4]; float mref[4];
  bool exact = false;
  for (int attempt = 0; attempt < 2; ++attempt) {
    float mrow[4];
    if (!exact) {
      const KFrag k0 = lK(0, 0);
#pragma unroll
      for (int qt = 0; qt < 4; ++qt) {
        f32x16 s = mfma32k8(k0.t, __builtin_bit_cast(s16x4, qtl[qt]), mfma32(k0.m, qm[qt], f32x16{}));
        float m = s[0];
#pragma unroll
        for (int i = 1; i < 16; ++i) m = fmaxf(m, s[i]);
        mrow[qt] = fmaxf(m, __shfl_xor(m, 32));
      }
    } else {
#pragma unroll
      for (int qt = 0; qt < 4; ++qt) mrow[qt] = -3.0e38f;
      for (int n = 0; n < 16; ++n) {
        asm volatile("s_waitcnt lgkmcnt(0)" ::: "memory");
        need(n); DMA_ALL(n, 0); wait_vm<0>();
        for (int t = 0; t < 2; ++t) {
          const KFrag k0 = lK(0, t);
#pragma unroll
          for (int qt = 0; qt < 4; ++qt) {
            f32x16 s = mfma32k8(k0.t, __builtin_bit_cast(s16x4, qtl[qt]), mfma32(k0.m, qm[qt], f32x16{}));
#pragma unroll
            for (int i = 0; i < 16; ++i) mrow[qt] = fmaxf(mrow[qt], s[i]);
          }
        }
      }
#pragma unroll
      for (int qt = 0; qt < 4; ++qt) mrow[qt] = fmaxf(mrow[qt], __shfl_xor(mrow[qt], 32));
      asm volatile("s_waitcnt lgkmcnt(0)" ::: "memory");
      nextc = 0; { const int c0 = pick(); DMA_CH(c0, 0); const int c1 = pick(); DMA_CH(c1, ASLOT); } wait_vm<5>();
    }
#pragma unroll
    for (int qt = 0; qt < 4; ++qt) {
      const float nm = -mrow[qt];
      const unsigned hi = cvtpk(nm, 0.f) & 0xffffu; const float r1 = nm - __uint_as_float(hi << 16);
      const unsigned mid = cvtpk(r1, 0.f) & 0xffffu; const float r2 = r1 - __uint_as_float(mid << 16);
      const unsigned lo = cvtpk(r2, 0.f) & 0xffffu;
      mref[qt] = -(__uint_as_float(hi << 16) + __uint_as_float(mid << 16) + __uint_as_float(lo << 16));
      qa[qt] = __builtin_bit_cast(s16x4, h ? u32x2{hi | (mid << 16), lo} : qtl[qt]);
      o[qt] = f32x16{};
    }
#define EXP2(x) __builtin_amdgcn_exp2f(x)
#define EXI(i) S[cur][i] = EXP2(S[cur][i])
#define CVI(j) P[cur][(j) >> 2][(j) & 3] = cvtpk(S[cur][2 * (j)], S[cur][2 * (j) + 1])
#define HALF(cur_, KN, qn, VP, qp, X0, X1, X2, X3) do { constexpr int cur = cur_; \
    SBAR(); \
    P[cur ^ 1][1][2] = cvtpk(S[cur ^ 1][12], S[cur ^ 1][13]); P[cur ^ 1][1][3] = cvtpk(S[cur ^ 1][14], S[cur ^ 1][15]); PIN(P[cur ^ 1][1]); \
    SBAR(); \
    S[cur ^ 1] = mfma32(KN.m, qm[qn], f32x16{}); SBAR(); X0; \
    EXI(0); EXI(1); EXI(2); EXI(3); EXI(4); EXI(5); PIN(S[cur]); \
    SBAR(); \
    S[cur ^ 1] = mfma32k8(KN.t, qa[qn], S[cur ^ 1]); SBAR(); X1; \
    CVI(0); EXI(6); CVI(1); EXI(7); EXI(8); EXI(9); PIN(S[cur]); PIN(P[cur][0]); \
    SBAR(); \
    o[qp] = mfma32(VP.a, __builtin_bit_cast(bf16x8, P[cur ^ 1][0]), o[qp]); SBAR(); X2; \
    CVI(2); EXI(10); CVI(3); EXI(11); EXI(12); EXI(13); PIN(S[cur]); PIN(P[cur][0]); \
    SBAR(); \
    o[qp] = mfma32(VP.b, __builtin_bit_cast(bf16x8, P[cur ^ 1][1]), o[qp]); SBAR(); X3; \
    CVI(4); EXI(14); CVI(5); EXI(15); PIN(P[cur][1]); \
    SBAR(); \
  } while (0)

    {
      f32x16 S[2]; u32x4 P[2][2]; KFrag kf[2]; VFrag vf[2];
      kf[0] = lK(0, 0);
      vf[1] = lV(0, 0);
      S[0] = mfma32k8(kf[0].t, qa[0], mfma32(kf[0].m, qm[0], f32x16{}));
      S[1] = f32x16{};
      P[1][0] = u32x4{0, 0, 0, 0}; P[1][1] = u32x4{0, 0, 0, 0};
      const unsigned gb = WS_KV + (head * 32 + kh) * KVCH, db = lds0 + ring;
#define PIECE_M0(IMM, g, d) asm volatile("s_mov_b32 m0, %0\n\ts_nop 0\n\tbuffer_load_dwordx4 %1, %2, %3 offen offset:" #IMM SC1L " lds" :: "s"(d), "v"(l16), "s"(rws), "s"(g) : "memory", "m0")
#define PIECE(IMM, g) asm volatile("buffer_load_dwordx4 %0, %1, %2 offen offset:" #IMM SC1L " lds" :: "v"(l16), "s"(rws), "s"(g) : "memory")
#define PERIOD(n, SC, SN, SP) do { \
        const int ch_ = pick(); \
        const unsigned g0 = gb + ch_ * (2 * KVCH), g1 = g0 + 4096, d0 = db + (SP) * ASLOT, d1 = d0 + 4096; \
        HALF(0, kf[0], 1, vf[1], 3, (vf[0] = lV((SC) * ASLOT, 0)), (void)0, (void)0, PIECE_M0(0, g0, d0)); \
        HALF(1, kf[0], 2, vf[0], 0, (kf[1] = lK((SC) * ASLOT, 1)), (void)0, (void)0, PIECE(1024, g0)); \
        HALF(0, kf[0], 3, vf[0], 1, (void)0, (void)0, (void)0, PIECE(2048, g0)); \
        HALF(1, kf[1], 0, vf[0], 2, (void)0, (void)0, (void)0, PIECE(3072, g0)); \
        HALF(0, kf[1], 1, vf[0], 3, (vf[1] = lV((SC) * ASLOT, 1)), (void)0, (void)0, PIECE_M0(0, g1, d1)); \
        wait_vm<5>();                                        \
        HALF(1, kf[1], 2, vf[1], 0, (kf[0] = lK((SN) * ASLOT, 0)), (void)0, (void)0, (void)0); \
        HALF(0, kf[1], 3, vf[1], 1, (void)0, (void)0, (void)0, (void)0); \
        HALF(1, kf[0], 0, vf[1], 2, (void)0, (void)0, (void)0, (void)0); \
      } while (0)
      for (int n = 0; n < 15; n += 3) { PERIOD(n, 0, 1, 2); PERIOD(n + 1, 1, 2, 0); PERIOD(n + 2, 2, 0, 1); }
      PERIOD(15, 0, 1, 2);
      wait_vm<0>();
      P[1][1][2] = cvtpk(S[1][12], S[1][13]); P[1][1][3] = cvtpk(S[1][14], S[1][15]);
      o[3] = mfma32(vf[1].a, __builtin_bit_cast(bf16x8, P[1][0]), o[3]);
      o[3] = mfma32(vf[1].b, __builtin_bit_cast(bf16x8, P[1][1]), o[3]);
    }

    auto send = [&](const f32x16& a, float m, int i) {
      const lds_ptr p = ldw + ring + i * 4096 + lane * 16;
      *(LAS f32x4*)(p) = f32x4{a[0], a[1], a[2], a[3]}; *(LAS f32x4*)(p + 1024) = f32x4{a[4], a[5], a[6], a[7]};
      *(LAS f32x4*)(p + 2048) = f32x4{a[8], a[9], a[10], a[11]}; *(LAS float*)(p + 3072) = m;
    };
    if (kh == 0) { send(o[2], mref[2], 0); send(o[3], mref[3], 1); } else { send(o[0], mref[0], 0); send(o[1], mref[1], 1); }
    const bool bad = h == 1 && !(o[0][8] <= 1.0e9f && o[1][8] <= 1.0e9f && o[2][8] <= 1.0e9f && o[3][8] <= 1.0e9f);
    const bool anybad = __any(bad);
    if (lane == 0) *(LAS unsigned*)(ldw + XCH + w * 4) = anybad ? 1u : 0u;
    bar();
    const u32x4 fl = *(const LAS u32x4*)(lds + XCH);
    if (exact || rfl(fl[0] | fl[1] | fl[2] | fl[3]) == 0u) break;
    bar();
    if (tid < 12) { *(LAS u32x4*)(ldw + tid * ASLOT + KVCH) = u32x4{ONE2, 0x00003F80u, 0u, 0u}; *(LAS u32x4*)(ldw + tid * ASLOT + KVCH + 16) = u32x4{ONE2, ONE2, ONE2, ONE2}; *(LAS u32x4*)(ldw + tid * ASLOT + KVCH + 32) = u32x4{0u, 0u, 0u, 0u}; }
    bar();
    exact = true;
  }
  __builtin_amdgcn_s_setprio(3);
  auto fin = [&](const f32x16& a, float ma, int i, int qt) {
    const lds_cptr p = lds + (w ^ 1) * ARING + i * 4096 + lane * 16;
    const f32x4 b0 = *(const LAS f32x4*)(p), b1 = *(const LAS f32x4*)(p + 1024), b2 = *(const LAS f32x4*)(p + 2048);
    const float mb = *(const LAS float*)(p + 3072);
    const float M = fmaxf(ma, mb), fa = EXP2(ma - M), fb = EXP2(mb - M);
    float v[12];
#pragma unroll
    for (int e = 0; e < 4; ++e) { v[e] = fa * a[e] + fb * b0[e]; v[4 + e] = fa * a[4 + e] + fb * b1[e]; v[8 + e] = fa * a[8 + e] + fb * b2[e]; }
    const float inv = __builtin_amdgcn_rcpf(__shfl(v[8], 32 + r32));
    const unsigned ob = WS_O + ((rb * 4 + qt) * 80 + pr * 5) * 512 + r32 * 16;
#pragma unroll
    for (int j = 0; j < 3; ++j) {
      const int sl = 2 * j + h, G = hh ? (sl == 4 ? 5 : 6 + sl) : sl;
      if (sl < 5) st8_sc1(rws, ob + (G >> 1) * 512 + (G & 1) * 8, u32x2{cvtpk(v[4 * j] * inv, v[4 * j + 1] * inv), cvtpk(v[4 * j + 2] * inv, v[4 * j + 3] * inv)});
    }
  };
  if (kh == 0) { fin(o[0], mref[0], 0, 0); fin(o[1], mref[1], 1, 1); } else { fin(o[2], mref[2], 0, 2); fin(o[3], mref[3], 1, 3); }
  wait_vm<0>();
  if (lane == 0) __hip_atomic_store((gu64*)(ws + WS_FO + (rb * 2 + kh) * 256 + head * 8), gen, RLX_AGENT);

}

#define LD16(d, rs, voff, soff, IMM) asm volatile("buffer_load_dwordx4 %0, %1, %2, %3 offen offset:" #IMM SC1L : "=v"(d) : "v"(voff), "s"(rs), "s"(soff) : "memory")

struct F2Ld { rsrc_t rws, rw; unsigned av, as, bv[3], bs, dA, dB[3]; int rot; };
template <int ST> DI void ffn2_issue(const F2Ld& L, int i) {
  constexpr unsigned slot = (ST & 3) * 18432;
  const unsigned st = rotk(ST, L.rot, 12);
  if (i < 2) dma16_sc1(L.rws, L.av, L.as + st * 2048 + i * 1024, L.dA + slot + i * 1024);
  else dma16(L.rw, L.bv[i - 2], L.bs + st * 32 * 1280, L.dB[i - 2] + slot);
}
struct F2Raw { bf16x8 a[2]; s16x4 b[10][2]; };
template <int ST, int I> DI void f2_ld(F2Raw& f, lds_cptr lds, int aoff, int boff) {
  const lds_cptr sb = lds + (ST & 3) * 18432;
  if constexpr (I < 2) f.a[I] = *(const LAS bf16x8*)(sb + aoff + I * 256);
  else f.b[(I - 2) >> 1][(I - 2) & 1] = trread(sb + boff + ((I - 2) & 1) * 4 * 320 + ((I - 2) >> 1) * 32);
}
template <int ST> DI void ffn2_step(f32x4 (&acc)[2][10], F2Raw (&fr)[2], const F2Ld& L, lds_cptr lds, int aoff, int boff) {
  if constexpr (ST + 1 < 12) { wait_vm<5 * ((10 - ST) < 2 ? (10 - ST) : 2)>(); bar(); }
  const F2Raw& f = fr[ST & 1]; F2Raw& n = fr[(ST + 1) & 1];
  constexpr bool NX = ST + 1 < 12;
#define F2L(i) do { if constexpr (NX) f2_ld<ST + 1, i>(n, lds, aoff, boff); } while (0)
#define F2I(i) do { if constexpr (ST + 4 < 12) ffn2_issue<ST + 4>(L, i); } while (0)
#define F2M(nt) do { const bf16x8 b_ = cat8(f.b[nt][0], f.b[nt][1]); acc[0][nt] = mfma16(b_, f.a[0], acc[0][nt]); acc[1][nt] = mfma16(b_, f.a[1], acc[1][nt]); SBAR(); } while (0)
  SBAR();
  F2M(0); F2L(0); F2L(1); F2L(2); SBAR();
  F2M(1); F2L(3); F2L(4); F2I(0); SBAR();
  F2M(2); F2L(5); F2L(6); F2L(7); SBAR();
  F2M(3); F2L(8); F2L(9); F2I(1); SBAR();
  F2M(4); F2L(10); F2L(11); F2L(12); SBAR();
  F2M(5); F2L(13); F2L(14); F2I(2); SBAR();
  F2M(6); F2L(15); F2L(16); F2L(17); SBAR();
  F2M(7); F2L(18); F2L(19); F2I(3); SBAR();
  F2M(8); F2L(20); F2L(21); SBAR();
  F2M(9); F2I(4); SBAR();
}
template <int... J> DI void f2_ld_all0(iseq<J...>, F2Raw& f, lds_cptr lds, int aoff, int boff) { (f2_ld<0, J>(f, lds, aoff, boff), ...); }
template <int... I> DI void ffn2_all(iseq<I...>, f32x4 (&acc)[2][10], const F2Ld& L, lds_cptr lds, int aoff, int boff) {
  F2Raw fr[2];
  wait_vm<15>(); bar();
  f2_ld_all0(mkseq<22>::type{}, fr[0], lds, aoff, boff);
  (ffn2_step<I>(acc, fr, L, lds, aoff, boff), ...);
}

DI void role_g(const void* x, const void* wo, const void* w2, const void* gw, char* ws, void* out, char* ldsg, int b) {
  const u64 gv = __hip_atomic_load((gu64*)(ws + WS_GEN), RLX_AGENT) + 1;
  const u64 gen = ((u64)rfl((unsigned)(gv >> 32)) << 32) | rfl((unsigned)gv);
  __builtin_amdgcn_s_setprio(0);
  const int tid = threadIdx.x, lane = tid & 63, w = rfl(tid >> 6), r32 = lane & 31, h = lane >> 5;
  const int j8 = b & 7, bi = (b - 256) >> 3, rg = j8 & 3, cg = j8 >> 2;
  const unsigned lds0 = (unsigned)(uintptr_t)ldsg;
  const lds_cptr lds = (lds_cptr)ldsg; const lds_ptr ldw = (lds_ptr)ldsg;
  const rsrc_t rws = mkrs(ws, WS_END);
  {
    const int rb = rg * 4 + (bi & 3), cb = cg * 8 + (bi >> 2);
    {
      const int row = rb * 128 + 8 * cb + 2 * w + h;
      float s = 0.f;
#pragma unroll
      for (int i = 0; i < 3; ++i) { const int c = r32 + 32 * i;
        if (c < 80) { const bf16x8 a = *(const bf16x8*)((const char*)x + row * 1280 + c * 16), g = *(const bf16x8*)((const char*)gw + c * 16);
#pragma unroll
          for (int e = 0; e < 8; ++e) s += (float)a[e] * (float)g[e]; } }
#pragma unroll
      for (int d = 1; d < 32; d <<= 1) s += __shfl_xor(s, d);
      if (r32 == 0) __hip_atomic_store((GAS unsigned*)(ws + WS_GATE + row * 4), __float_as_uint(1.f / (1.f + __expf(-s))), RLX_AGENT);
    }
    publish(ws, WS_FG + rb * 128 + cb * 8, gen);

  }
  __builtin_amdgcn_s_setprio(0);
  {
    const int rb = rg * 4 + (bi & 3), kq = 2 * cg + ((bi >> 2) & 1), c4 = bi >> 3;
    F2Ld L; L.rws = rws; L.rw = mkrs(w2, F_ * D_ * 2);
    L.av = lane * 16; L.as = WS_H + ((rb * 4 + w) * 192 + kq * 48) * 512; L.dA = lds0 + w * 2048;
#pragma unroll
    for (int i = 0; i < 3; ++i) { const int j = (w + 4 * i < 10) ? w + 4 * i : w + 4, idx = j * 64 + lane, k = idx / 20, pc = idx - k * 20;
      L.bv[i] = k * 1280 + c4 * 320 + pc * 16; L.dB[i] = lds0 + 8192 + j * 1024; }
    L.bs = kq * 384 * 1280; L.rot = 3 * (((bi >> 3) + (bi & 3)) & 3);
    const int aoff = w * 2048 + (lane >> 4) * 512 + (lane & 15) * 16;
    const int boff = 8192 + (8 * (lane >> 4) + ((lane & 15) >> 2)) * 320 + 8 * (lane & 3);
    bar();
    wait_line_blk(ws, WS_FH + (rb * 4 + kq) * 128, gen, 0xffffu);

#pragma unroll
    for (int i = 0; i < 5; ++i) ffn2_issue<0>(L, i);
#pragma unroll
    for (int i = 0; i < 5; ++i) ffn2_issue<1>(L, i);
#pragma unroll
    for (int i = 0; i < 5; ++i) ffn2_issue<2>(L, i);
#pragma unroll
    for (int i = 0; i < 5; ++i) ffn2_issue<3>(L, i);
    f32x4 acc[2][10];
#pragma unroll
    for (int mt = 0; mt < 2; ++mt)
#pragma unroll
      for (int nt = 0; nt < 10; ++nt) acc[mt][nt] = f32x4{};
    ffn2_all(mkseq<12>::type{}, acc, L, lds, aoff, boff);
    const unsigned pbase = WS_PB + (((kq * 32 + rb * 2 + (w >> 1)) * 8 + 2 * c4) * 4 + (w & 1) * 2) * 2560;
#pragma unroll
    for (int mt = 0; mt < 2; ++mt)
#pragma unroll
      for (int nt = 0; nt < 10; ++nt)
        __builtin_amdgcn_raw_buffer_store_b64(u32x2{cvtpk(acc[mt][nt][0], acc[mt][nt][1]), cvtpk(acc[mt][nt][2], acc[mt][nt][3])}, rws, lane * 8,
                                              (int)(pbase + mt * 2560 + (nt / 5) * 4 * 2560 + (nt % 5) * 512), 16);
    publish(ws, WS_FB + rb * 128 + (kq * 4 + c4) * 8, gen);

  }
  const int r64 = rg * 8 + (bi & 7), c80 = cg * 4 + (bi >> 3), rb = r64 >> 1;
  const unsigned ring = lds0 + w * 20480;
  const lds_cptr lring = lds + w * 20480;
  unsigned bv[5];
#pragma unroll
  for (int j = 0; j < 5; ++j) { const int idx = j * 64 + lane, k = idx / 10, pc = idx - k * 10; bv[j] = k * 1280 + c80 * 160 + pc * 16; }
  const int boff = (8 * (lane >> 4) + ((lane & 15) >> 2)) * 160 + 8 * (lane & 3);
  const unsigned avoff = (lane >> 4) * 512 + (lane & 15) * 16;
  f32x4 acc[4][5]; unsigned bpk[5][2];
  auto issueB = [&](rsrc_t rs, int k0, int i) {
#pragma unroll
    for (int j = 0; j < 5; ++j) dma16(rs, bv[j], (k0 + i * 32) * 1280, ring + (i & 3) * 5120 + j * 1024);
  };
  auto compute = [&](int i, const u32x4 (&af)[4]) {
    bf16x8 bf[5];
#pragma unroll
    for (int nt = 0; nt < 5; ++nt) bf[nt] = cat8(trread(lring + (i & 3) * 5120 + boff + nt * 32), trread(lring + (i & 3) * 5120 + boff + 4 * 160 + nt * 32));
#pragma unroll
    for (int mt = 0; mt < 4; ++mt)
#pragma unroll
      for (int nt = 0; nt < 5; ++nt) acc[mt][nt] = mfma16(bf[nt], __builtin_bit_cast(bf16x8, af[mt]), acc[mt][nt]);
  };
  auto reduce_scatter = [&](f32x4 (&res)[5]) {
    asm volatile("s_waitcnt lgkmcnt(0)" ::: "memory");
#pragma unroll
    for (int mt = 0; mt < 4; ++mt)
#pragma unroll
      for (int nt = 0; nt < 5; ++nt) *(LAS f32x4*)(ldw + w * 20480 + (mt * 5 + nt) * 1024 + lane * 16) = acc[mt][nt];
    bar();
#pragma unroll
    for (int nt = 0; nt < 5; ++nt) {
      f32x4 s = *(const LAS f32x4*)(lds + (w * 5 + nt) * 1024 + lane * 16);
#pragma unroll
      for (int sl = 1; sl < 4; ++sl) s += *(const LAS f32x4*)(lds + sl * 20480 + (w * 5 + nt) * 1024 + lane * 16);
      res[nt] = s;
    }
  };
#define LDA(set, i) do { const unsigned o_ = (i) * 2048; LD16(af[set][0], rws, avoff, as0 + o_, 0); LD16(af[set][1], rws, avoff, as0 + o_, 256); LD16(af[set][2], rws, avoff, as1 + o_, 0); LD16(af[set][3], rws, avoff, as1 + o_, 256); } while (0)
#define WAITA(N, set) asm volatile("s_waitcnt vmcnt(" #N ") lgkmcnt(0)" : "+v"(af[set][0]), "+v"(af[set][1]), "+v"(af[set][2]), "+v"(af[set][3]) :: "memory")
  if (b == 511 && w == 0) {
    for (unsigned spins = 0; spins <= SPIN_CAP; ++spins) {
      bool ok = true;
#pragma unroll
      for (int i = 0; i < 4; ++i) ok &= __hip_atomic_load((gu64*)(ws + WS_FG + (i * 64 + lane) * 8), RLX_AGENT) == gen;
#pragma unroll
      for (int i = 0; i < 4; ++i) ok &= __hip_atomic_load((gu64*)(ws + WS_HINT + (i * 64 + lane) * 8), RLX_AGENT) == gen;
      if (__all(ok)) break;
      __builtin_amdgcn_s_sleep(8);
    }
    if (lane == 0) __hip_atomic_store((gu64*)(ws + WS_GEN), gen, RLX_AGENT);
  }
  {
    const rsrc_t ro = mkrs(wo, D_ * D_ * 2);
    const int k0 = w * 160;
    issueB(ro, k0, 0); issueB(ro, k0, 1); issueB(ro, k0, 2); issueB(ro, k0, 3);
    {
      if (w == 0) wait_line(ws, WS_FG + rb * 128, gen, lane, 0xffffu);
      wait_line_blk(ws, WS_FB + rb * 128, gen, 0x1111u << (c80 >> 1));
      const unsigned pb0 = WS_PB + ((r64 * 8 + c80) * 4 + w) * 2560;
#pragma unroll
      for (int nt = 0; nt < 5; ++nt) {
        f32x4 sum = {0.f, 0.f, 0.f, 0.f};
#pragma unroll
        for (int kq = 0; kq < 4; ++kq) {
          const u32x2 p = __builtin_bit_cast(u32x2, __builtin_amdgcn_raw_buffer_load_b64(rws, lane * 8, (int)(pb0 + kq * 32 * 8 * 4 * 2560 + nt * 512), AUXL));
          sum += f32x4{__uint_as_float(p.x << 16), __uint_as_float(p.x & 0xffff0000u), __uint_as_float(p.y << 16), __uint_as_float(p.y & 0xffff0000u)};
        }
        bpk[nt][0] = cvtpk(sum[0], sum[1]); bpk[nt][1] = cvtpk(sum[2], sum[3]);
      }

    }
    const float g = __uint_as_float(__hip_atomic_load((GAS unsigned*)(ws + WS_GATE + (r64 * 64 + w * 16 + (lane & 15)) * 4), RLX_AGENT));
#pragma unroll
    for (int mt = 0; mt < 4; ++mt)
#pragma unroll
      for (int nt = 0; nt < 5; ++nt) acc[mt][nt] = f32x4{};
    wait_line(ws, WS_FO + r64 * 256 + (w >> 1) * 128, gen, lane, 0xffu << ((w & 1) * 8));

    u32x4 af[5][4];
    const unsigned as0 = WS_O + (r64 * 2 * 80 + w * 20) * 512, as1 = as0 + 80 * 512;
    LDA(0, 0); LDA(1, 1); LDA(2, 2); LDA(3, 3); LDA(4, 4);
    WAITA(16, 0); compute(0, af[0]);
    asm volatile("s_waitcnt lgkmcnt(0)" ::: "memory");
    issueB(ro, k0, 4);
    WAITA(17, 1); compute(1, af[1]);
    WAITA(13, 2); compute(2, af[2]);
    WAITA(9, 3); compute(3, af[3]);
    WAITA(0, 4); compute(4, af[4]);

    f32x4 asum[5];
    reduce_scatter(asum);

    const int row = r64 * 64 + w * 16 + (lane & 15);
    const rsrc_t rout = mkrs(out, S_ * D_ * 2);
#pragma unroll
    for (int nt = 0; nt < 5; ++nt) {
      const f32x4 bb = {__uint_as_float(bpk[nt][0] << 16), __uint_as_float(bpk[nt][0] & 0xffff0000u), __uint_as_float(bpk[nt][1] << 16), __uint_as_float(bpk[nt][1] & 0xffff0000u)};
      const f32x4 v = g * asum[nt] + (1.f - g) * bb;
      __builtin_amdgcn_raw_buffer_store_b64(u32x2{cvtpk(v[0], v[1]), cvtpk(v[2], v[3])}, rout, row * 1280 + (c80 * 80 + nt * 16 + 4 * (lane >> 4)) * 2, 0, 0);
    }

  }
}

__global__ __launch_bounds__(256, 2) void k_fused(const void* x, const void* wqkv, const void* wo, const void* w1, const void* w2, const void* gw, char* ws, void* out) {
  __shared__ __attribute__((aligned(16))) char lds[LDS_BYTES];
  const int b = blockIdx.x;

  if (b < 256) role_a(x, wqkv, w1, ws, lds, b);
  else role_g(x, wo, w2, gw, ws, out, lds, b);
}

extern "C" void kernel_launch(void* const* d_in, const int* in_sizes, int n_in, void* d_out, int out_size, void* d_ws, size_t ws_size, hipStream_t stream) {
  k_fused<<<512, 256, 0, stream>>>(d_in[0], d_in[1], d_in[2], d_in[4], d_in[5], d_in[6], (char*)d_ws, d_out);
}
```
